```python
import torch

def _softmax(x, axis=-1):
    return torch.softmax(x, dim=axis)
def _gelu(x):
    return torch.nn.functional.gelu(x, approximate="tanh")
def _silu(x):
    return torch.nn.functional.silu(x)
def _sigmoid(x):
    return torch.sigmoid(x)
def _layernorm(x, axis=-1, eps=1e-5):
    m = x.mean(dim=axis, keepdim=True)
    v = x.var(dim=axis, keepdim=True, unbiased=False)
    return (x - m) / torch.sqrt(v + eps)
def _scatter_add(tgt, idx, src, axis=0):
    return tgt.clone().index_add_(axis, idx, src)
def _segment_sum(src, seg, n):
    out = torch.zeros((n,) + src.shape[1:], dtype=src.dtype)
    return out.index_add_(0, seg, src)
def _pad(x, pw):
    flat = tuple(v for pair in reversed(pw) for v in pair)
    return torch.nn.functional.pad(x, flat)

kernel_name = 'interleave_layers__sliding_block_cached__swiglu_ffn__decode'

def setup_inputs():

    _w = (lambda a: (lambda t: t.to(torch.bfloat16) if t.is_floating_point() else t)(torch.as_tensor(a)))
    def _A_setup():
        B, Sq, D = 1, 1, 448
        Hq, Hkv, Sk, Dh = 16, 8, 65536, 256
        assert Hq % Hkv == 0, (Hq, Hkv)
        return {
            "x": _w((lambda *s: torch.randn(*s, dtype=torch.bfloat16) * 0.05)(B, Sq, D)),
            "k_cache": _w((lambda *s: torch.randn(*s, dtype=torch.bfloat16) * 1.6)(B, Hkv, Sk, Dh)),
            "v_cache": _w((lambda *s: torch.randn(*s, dtype=torch.bfloat16) * 1.6)(B, Hkv, Sk, Dh)),
            "wq": _w((lambda *s: torch.randn(*s, dtype=torch.bfloat16) * 1.6)(D, Hq * Dh) * (D ** -0.5)),
            "wo": _w((lambda *s: torch.randn(*s, dtype=torch.bfloat16) * 1.6)(Hq * Dh, D) * ((Hq * Dh) ** -0.5)),
        }
    _A_in = _A_setup()

    def _B_setup():
        B, S, D, F = 1, 1, 448, 1792
        return {
            "x": _w((lambda *s: torch.randn(*s, dtype=torch.bfloat16) * 0.05)(B, S, D)),
            "w_gate": _w((lambda *s: torch.randn(*s, dtype=torch.bfloat16) * 0.05)(D, F)),
            "w_up": _w((lambda *s: torch.randn(*s, dtype=torch.bfloat16) * 0.05)(D, F)),
            "w_down": _w((lambda *s: torch.randn(*s, dtype=torch.bfloat16) * 0.05)(F, D)),
        }
    _B_in = _B_setup()

    return {
        "A_x": _A_in["x"],
        "A_k_cache": _A_in["k_cache"],
        "A_v_cache": _A_in["v_cache"],
        "A_wq": _A_in["wq"],
        "A_wo": _A_in["wo"],
        "B_x": _B_in["x"],
        "B_w_gate": _B_in["w_gate"],
        "B_w_up": _B_in["w_up"],
        "B_w_down": _B_in["w_down"],
    }

def reference(A_x, A_k_cache, A_v_cache, A_wq, A_wo, B_x, B_w_gate, B_w_up, B_w_down):
    def _A_ref(x, k_cache, v_cache, wq, wo):
        B, Sq, D = x.shape
        Hq, Dh = 16, 256
        Hkv, Sk = k_cache.shape[1], k_cache.shape[2]
        W = min(4096, max(256, Sk // 4))
        rep = Hq // Hkv
        q = (lambda x, *d: x.permute(*d))((x @ wq).reshape(B, Sq, Hq, Dh), 0, 2, 1, 3)
        k = (lambda x, n, axis: torch.repeat_interleave(x, n, dim=axis))(k_cache[:, :, Sk - W:, :], rep, axis=1)
        v = (lambda x, n, axis: torch.repeat_interleave(x, n, dim=axis))(v_cache[:, :, Sk - W:, :], rep, axis=1)
        scores = torch.einsum("bhqd,bhkd->bhqk", q, k) * (Dh ** -0.5)
        p = _softmax(scores, axis=-1)
        o = torch.einsum("bhqk,bhkd->bhqd", p, v)
        return (lambda x, *d: x.permute(*d))(o, 0, 2, 1, 3).reshape(B, Sq, Hq * Dh) @ wo

    def _B_ref(x, w_gate, w_up, w_down):
        g = _silu(x @ w_gate)
        u = x @ w_up
        return (g * u) @ w_down

    x = A_x
    x = x + _A_ref(_layernorm(x, axis=-1), A_k_cache, A_v_cache, A_wq, A_wo)
    x = x + _B_ref(_layernorm(x, axis=-1), B_w_gate, B_w_up, B_w_down)
    x = x + _A_ref(_layernorm(x, axis=-1), A_k_cache, A_v_cache, A_wq, A_wo)
    x = x + _B_ref(_layernorm(x, axis=-1), B_w_gate, B_w_up, B_w_down)
    return x
```

```cpp
#include <hip/hip_runtime.h>
#include <hip/hip_bf16.h>
#include <stdint.h>

#define MIMIC 0

typedef unsigned short u16;
__device__ __forceinline__ float bf2f(u16 b) { return __uint_as_float(((unsigned)b) << 16); }
__device__ __forceinline__ float rbf(float f) {
  unsigned u = __float_as_uint(f); u = (u + 0x7fffu + ((u >> 16) & 1u)) & 0xffff0000u; return __uint_as_float(u);
}
__device__ __forceinline__ float mr(float f) { return MIMIC ? rbf(f) : f; }

constexpr int D = 448, HQ = 16, HKV = 8, SK = 65536, DH = 256, W = 4096, F = 1792;

__device__ void ln_block(const float* x, float* sh, float* red) {
  int t = threadIdx.x;
  float s = 0.f;
  for (int i = t; i < D; i += blockDim.x) s += x[i];
  red[t] = s; __syncthreads();
  for (int o = blockDim.x / 2; o > 0; o >>= 1) { if (t < o) red[t] += red[t + o]; __syncthreads(); }
  float mean = red[0] / D; __syncthreads();
  float s2 = 0.f;
  for (int i = t; i < D; i += blockDim.x) { float d = x[i] - mean; s2 += d * d; }
  red[t] = s2; __syncthreads();
  for (int o = blockDim.x / 2; o > 0; o >>= 1) { if (t < o) red[t] += red[t + o]; __syncthreads(); }
  float var = red[0] / D; __syncthreads();
  float m = mr(mean), v = mr(var);
  float den = mr(sqrtf(mr(v + 1e-5f)));
  for (int i = t; i < D; i += blockDim.x) sh[i] = mr(mr(x[i] - m) / den);
  __syncthreads();
}

__global__ void k_init(const u16* x, float* xs) { int i = threadIdx.x; if (i < D) xs[i] = bf2f(x[i]); }

__global__ void k_q(const float* xs, const u16* wq, float* q) {
  __shared__ float sh[D]; __shared__ float red[256];
  ln_block(xs, sh, red);
  int c = blockIdx.x * 256 + threadIdx.x;
  float a = 0.f;
  for (int k = 0; k < D; ++k) a += sh[k] * bf2f(wq[(size_t)k * 4096 + c]);
  q[c] = mr(a);
}

__global__ void k_attn(const float* q, const u16* kc, const u16* vc, float* po, float* pml) {
  __shared__ float qs[512]; __shared__ float sc[256]; __shared__ float mx[2], ls[2];
  int b = blockIdx.x, kvh = b / 32, sp = b % 32, t = threadIdx.x;
  for (int i = t; i < 512; i += 256) qs[i] = q[kvh * 512 + i];
  __syncthreads();
  int h = t / 128, key = t % 128;
  const u16* kr = kc + ((size_t)kvh * SK + (SK - W) + sp * 128 + key) * DH;
  float a = 0.f;
  for (int d = 0; d < DH; ++d) a += qs[h * 256 + d] * bf2f(kr[d]);
  a = mr(a) * 0.0625f;
  sc[t] = a; __syncthreads();
  if (t < 2) { float m = -INFINITY; for (int i = 0; i < 128; ++i) m = fmaxf(m, sc[t * 128 + i]); mx[t] = m; }
  __syncthreads();
  float p = __expf(a - mx[h]); sc[t] = p; __syncthreads();
  if (t < 2) { float s = 0; for (int i = 0; i < 128; ++i) s += sc[t * 128 + i]; ls[t] = s; }
  __syncthreads();
  const u16* vr = vc + ((size_t)kvh * SK + (SK - W) + sp * 128) * DH;
  float o0 = 0.f, o1 = 0.f;
  for (int k = 0; k < 128; ++k) { float v = bf2f(vr[(size_t)k * DH + t]); o0 += sc[k] * v; o1 += sc[128 + k] * v; }
  po[((size_t)(kvh * 2 + 0) * 32 + sp) * 256 + t] = o0;
  po[((size_t)(kvh * 2 + 1) * 32 + sp) * 256 + t] = o1;
  if (t < 2) { pml[((kvh * 2 + t) * 32 + sp) * 2] = mx[t]; pml[((kvh * 2 + t) * 32 + sp) * 2 + 1] = ls[t]; }
}

__global__ void k_comb(const float* po, const float* pml, float* o) {
  int h = blockIdx.x, t = threadIdx.x;
  float M = -INFINITY;
  for (int s = 0; s < 32; ++s) M = fmaxf(M, pml[(h * 32 + s) * 2]);
  float L = 0.f, a = 0.f;
  for (int s = 0; s < 32; ++s) { float w = __expf(pml[(h * 32 + s) * 2] - M); L += w * pml[(h * 32 + s) * 2 + 1]; a += w * po[((size_t)h * 32 + s) * 256 + t]; }
  o[h * 256 + t] = mr(a / L);
}

__global__ void k_down(const float* in, int K, const u16* w, float* xs, u16* outbf) {
  __shared__ float red[256];
  int t = threadIdx.x, c = blockIdx.x * 64 + (t & 63), g = t >> 6;
  float a = 0.f;
  for (int k = g; k < K; k += 4) a += in[k] * bf2f(w[(size_t)k * D + c]);
  red[t] = a; __syncthreads();
  if (t < 64) { float s = red[t] + red[t + 64] + red[t + 128] + red[t + 192]; float r = mr(xs[c] + mr(s)); xs[c] = r;
    if (outbf) { outbf[c] = (u16)(__float_as_uint(rbf(r)) >> 16); } }
}

__global__ void k_gu(const float* xs, const u16* wg, const u16* wu, float* hbuf) {
  __shared__ float sh[D]; __shared__ float red[256];
  ln_block(xs, sh, red);
  int c = blockIdx.x * 256 + threadIdx.x;
  float g = 0.f, u = 0.f;
  for (int k = 0; k < D; ++k) { g += sh[k] * bf2f(wg[(size_t)k * F + c]); u += sh[k] * bf2f(wu[(size_t)k * F + c]); }
  g = mr(g); u = mr(u);
  float s = mr(g / (1.f + __expf(-g)));
  hbuf[c] = mr(s * u);
}

extern "C" void kernel_launch(void* const* d_in, const int* in_sizes, int n_in,
                              void* d_out, int out_size, void* d_ws, size_t ws_size,
                              hipStream_t stream) {
  const u16* x = (const u16*)d_in[0];
  const u16* kc = (const u16*)d_in[1];
  const u16* vc = (const u16*)d_in[2];
  const u16* wq = (const u16*)d_in[3];
  const u16* wo = (const u16*)d_in[4];
  const u16* wg = (const u16*)d_in[6];
  const u16* wu = (const u16*)d_in[7];
  const u16* wd = (const u16*)d_in[8];
  float* ws = (float*)d_ws;
  float* xs = ws; float* q = ws + 1024; float* o = q + 4096; float* hb = o + 4096; float* pml = hb + 2048; float* po = pml + 2048;
  k_init<<<1, 512, 0, stream>>>(x, xs);
  for (int l = 0; l < 2; ++l) {
    k_q<<<16, 256, 0, stream>>>(xs, wq, q);
    k_attn<<<256, 256, 0, stream>>>(q, kc, vc, po, pml);
    k_comb<<<16, 256, 0, stream>>>(po, pml, o);
    k_down<<<7, 256, 0, stream>>>(o, 4096, wo, xs, nullptr);
    k_gu<<<7, 256, 0, stream>>>(xs, wg, wu, hb);
    k_down<<<7, 256, 0, stream>>>(hb, F, wd, xs, l == 1 ? (u16*)d_out : nullptr);
  }
}
```

```cpp
#include <hip/hip_runtime.h>
#include <stdint.h>
#pragma clang diagnostic ignored "-Wint-to-pointer-cast"

typedef unsigned short u16;
typedef unsigned long long u64;
typedef unsigned v4u __attribute__((ext_vector_type(4)));
typedef unsigned v2u __attribute__((ext_vector_type(2)));
typedef float v4f __attribute__((ext_vector_type(4)));
typedef float v2f __attribute__((ext_vector_type(2)));
typedef short v4s __attribute__((ext_vector_type(4)));
typedef __bf16 v8bf __attribute__((ext_vector_type(8)));
typedef __bf16 v2bf __attribute__((ext_vector_type(2)));
#define LAS __attribute__((address_space(3)))
#define GAS __attribute__((address_space(1)))
#define DEVI __device__ __forceinline__

#define STAMPS 0

namespace {
constexpr int D = 448, SK = 65536, WIN = 4096, FF = 1792;
constexpr unsigned WS_EPOCH = 0;
constexpr unsigned WS_QREC = 4096;
constexpr unsigned WS_QSTAT = WS_QREC + 8 * 7 * 2048;
constexpr unsigned WS_OREC = WS_QSTAT + 8 * 7 * 128;
constexpr unsigned WS_OSTAT = WS_OREC + 256 * 2048;
constexpr unsigned WS_XREC = WS_OSTAT + 256 * 128;
constexpr unsigned WS_X1 = WS_XREC + 16 * 1792;
constexpr unsigned WS_X2 = WS_X1 + 1792;
constexpr unsigned WS_DREC = WS_X2 + 1792;
constexpr unsigned WS_L = 1u << 20;
static_assert(WS_DREC + 112 * 1792 <= WS_L, "layer block");
constexpr unsigned WS_CDELTA = 2 * WS_L;
constexpr unsigned WS_STAMP = 4u << 20;
constexpr unsigned L_V = 0;
constexpr unsigned L_WQ = 65536;
constexpr unsigned L_W2 = 81920;
constexpr unsigned L_Q = L_W2 + 43008;
constexpr unsigned L_P = L_Q + 1280;
constexpr unsigned L_ZERO = L_P + 512;
constexpr unsigned L_S = L_ZERO + 16;
constexpr unsigned L_OP = L_S + 128;
constexpr unsigned L_OS = L_OP + 8 * 528;
constexpr unsigned L_XS = L_OS + 64;
constexpr unsigned L_RED = L_XS + 1024;
constexpr unsigned L_G = L_RED + 1792;
constexpr unsigned L_END = L_G + 7 * 144;
constexpr unsigned L_STAMP = (L_END + 15) / 16 * 16;
constexpr unsigned LDS_BYTES = L_STAMP + 256;
constexpr unsigned SPIN_CAP = 1u << 16;

struct Params { const u16 *x, *kc, *vc, *wq, *wo, *wg, *wu, *wd; u16* out; unsigned char* ws; };
typedef __amdgpu_buffer_rsrc_t rsrc_t;

DEVI float u2f(unsigned u) { return __builtin_bit_cast(float, u); }
DEVI unsigned f2u(float f) { return __builtin_bit_cast(unsigned, f); }
DEVI unsigned pk_bf16(float lo, float hi) { v2f f = {lo, hi}; return __builtin_bit_cast(unsigned, __builtin_convertvector(f, v2bf)); }
DEVI float rbf(float f) { return u2f(pk_bf16(0.f, f)); }
DEVI v4f mfma(v4u a, v4u b, v4f c) { return __builtin_amdgcn_mfma_f32_16x16x32_bf16(__builtin_bit_cast(v8bf, a), __builtin_bit_cast(v8bf, b), c, 0, 0, 0); }
DEVI float dot2(unsigned a, unsigned b, float c) { return __builtin_amdgcn_fdot2_f32_bf16(__builtin_bit_cast(v2bf, a), __builtin_bit_cast(v2bf, b), c, false); }
template <int CTRL> DEVI float dpp(float x) { return u2f((unsigned)__builtin_amdgcn_mov_dpp((int)f2u(x), CTRL, 0xf, 0xf, true)); }
constexpr int ROR1 = 0x121, ROR2 = 0x122, ROR4 = 0x124, ROR8 = 0x128, QP_X1 = 0xB1, QP_X2 = 0x4E;
DEVI float red32(float a, float b) { auto r = __builtin_amdgcn_permlane32_swap(f2u(a), f2u(b), false, false); return u2f(r[0]) + u2f(r[1]); }
DEVI float red16(float a, float b) { auto r = __builtin_amdgcn_permlane16_swap(f2u(a), f2u(b), false, false); return u2f(r[0]) + u2f(r[1]); }
DEVI float tred4(v4f a) { return red16(red32(a.x, a.z), red32(a.y, a.w)); }
DEVI float rows_max(float x) {
  auto r = __builtin_amdgcn_permlane32_swap(f2u(x), f2u(x), false, false); x = fmaxf(u2f(r[0]), u2f(r[1]));
  auto s = __builtin_amdgcn_permlane16_swap(f2u(x), f2u(x), false, false); return fmaxf(u2f(s[0]), u2f(s[1]));
}
DEVI float rows_sum(float x) { x = red32(x, x); return red16(x, x); }
DEVI float rdlane(float x, int lane) { return u2f((unsigned)__builtin_amdgcn_readlane((int)f2u(x), lane)); }
DEVI unsigned tagv(float v, unsigned tag) { return (f2u(v) & 0xffffff00u) | tag; }
DEVI v4u tag4(v4f v, unsigned tag) { v4u r = {tagv(v.x, tag), tagv(v.y, tag), tagv(v.z, tag), tagv(v.w, tag)}; return r; }
DEVI unsigned bad4(v4u x, unsigned tag) { return __builtin_amdgcn_bitop3_b32(x.x, x.y, tag, 0x7E) | __builtin_amdgcn_bitop3_b32(x.z, x.w, tag, 0x7E); }
DEVI v4f asf(v4u x) { return __builtin_bit_cast(v4f, x); }
#define BAR() asm volatile("s_waitcnt lgkmcnt(0)\n\ts_barrier" ::: "memory")
#define LGKM0() asm volatile("s_waitcnt lgkmcnt(0)" ::: "memory")
#define VMWAIT(n) asm volatile("s_waitcnt vmcnt(" #n ")" ::: "memory")
#define PIN(x) asm volatile("" : "+v"(x))

DEVI void dma16(const void* sbase, unsigned voff, unsigned ldsaddr) {
  unsigned keep;
  asm volatile("s_mov_b32 %0, m0\n\ts_mov_b32 m0, %3\n\ts_nop 3\n\tglobal_load_lds_dwordx4 %1, %2 nt\n\ts_mov_b32 m0, %0"
               : "=&s"(keep) : "v"(voff), "s"(sbase), "s"(ldsaddr) : "memory");
}
DEVI void dma16x4(const void* sbase, unsigned va, unsigned vb, unsigned ldsaddr) {
  unsigned keep;
  asm volatile("s_mov_b32 %0, m0\n\ts_mov_b32 m0, %4\n\ts_nop 3\n\t"
               "global_load_lds_dwordx4 %1, %3 nt\n\tglobal_load_lds_dwordx4 %2, %3 offset:1024 nt\n\t"
               "global_load_lds_dwordx4 %1, %3 offset:2048 nt\n\tglobal_load_lds_dwordx4 %2, %3 offset:3072 nt\n\ts_mov_b32 m0, %0"
               : "=&s"(keep) : "v"(va), "v"(vb), "s"(sbase), "s"(ldsaddr) : "memory");
}
template <int OFF> DEVI void ldk(v4u& dst, const void* sbase, unsigned voff) {
  asm volatile("global_load_dwordx4 %0, %1, %2 offset:%3 nt" : "=v"(dst) : "v"(voff), "s"(sbase), "i"(OFF) : "memory");
}
DEVI void ld_sc1_x4(v4u& dst, const void* sbase, unsigned voff) {
  asm volatile("global_load_dwordx4 %0, %1, %2 sc1" : "=v"(dst) : "v"(voff), "s"(sbase) : "memory");
}
DEVI v4u ws_ld4(rsrc_t rs, unsigned vo, unsigned so) { return __builtin_amdgcn_raw_buffer_load_b128(rs, vo, so, 16); }
DEVI unsigned ws_ld1(rsrc_t rs, unsigned vo, unsigned so) { return __builtin_amdgcn_raw_buffer_load_b32(rs, vo, so, 16); }
DEVI void ws_st4(rsrc_t rs, unsigned vo, unsigned so, v4u v) { __builtin_amdgcn_raw_buffer_store_b128(v, rs, vo, so, 16); }
DEVI void ws_st1(rsrc_t rs, unsigned vo, unsigned so, unsigned v) { __builtin_amdgcn_raw_buffer_store_b32(v, rs, vo, so, 16); }
DEVI v4u lds_ld4(unsigned a) { return *(LAS v4u*)a; }
DEVI float lds_ldf(unsigned a) { return *(LAS float*)a; }
DEVI v2u tr_read(unsigned a) { return __builtin_bit_cast(v2u, __builtin_amdgcn_ds_read_tr16_b64_v4i16((LAS v4s*)a)); }
DEVI v4u frag(unsigned a0, unsigned a1) { v2u lo = tr_read(a0), hi = tr_read(a1); v4u r = {lo.x, lo.y, hi.x, hi.y}; return r; }
DEVI bool allok(unsigned bad) { return __all((bad & 0xffu) == 0u); }

template <unsigned STR> DEVI void poll_4x4(rsrc_t rs, unsigned vo, unsigned so, unsigned tag, v4u (&x)[4]) {
  for (unsigned sp = 0; sp < SPIN_CAP; ++sp) {
    asm volatile("" ::: "memory");
#pragma unroll
    for (int k = 0; k < 4; ++k) x[k] = ws_ld4(rs, vo, so + k * STR);
    if (allok(bad4(x[0], tag) | bad4(x[1], tag) | bad4(x[2], tag) | bad4(x[3], tag))) break;
  }
}
DEVI void poll_2x4(rsrc_t rs, unsigned v0, unsigned v1, unsigned so, unsigned tag, v4u (&x)[2]) {
  for (unsigned sp = 0; sp < SPIN_CAP; ++sp) {
    asm volatile("" ::: "memory");
    x[0] = ws_ld4(rs, v0, so); x[1] = ws_ld4(rs, v1, so);
    if (allok(bad4(x[0], tag) | bad4(x[1], tag))) break;
  }
}
DEVI unsigned poll1(rsrc_t rs, unsigned vo, unsigned so, unsigned tag) {
  unsigned x = 0;
  for (unsigned sp = 0; sp < SPIN_CAP; ++sp) {
    asm volatile("" ::: "memory");
    x = ws_ld1(rs, vo, so);
    if (allok(x ^ tag)) break;
  }
  return x;
}
DEVI void ln_scalars(float sx, float sxx, float& m, float& rden) {
  const float mean = sx * (1.0f / D);
  const float var = fmaxf(sxx * (1.0f / D) - mean * mean, 0.f);
  m = rbf(mean);
  const float den = rbf(__builtin_amdgcn_sqrtf(rbf(rbf(var) + 1e-5f)));
  rden = __builtin_amdgcn_rcpf(den);
}

#define STAMP(i) do {} while (0)

struct Ctx {
  unsigned xs_w, xs_r, q_st, qo0, qo1, q_lw, qa, p_w, pa0, pa1, pa2, pa3;
};

DEVI void q_stage(rsrc_t rs, unsigned lay, unsigned tag, float xv, const v4u (&wqf)[2], unsigned xs_w, unsigned xs_r, unsigned q_st,
                  bool stat, unsigned stat_off, int l) {
  const v4u ones = {0x3f803f80u, 0x3f803f80u, 0x3f803f80u, 0x3f803f80u};
  const v4f z4 = {0.f, 0.f, 0.f, 0.f};
  *(LAS u16*)xs_w = (u16)(f2u(xv) >> 16);
  LGKM0();
  const v4u x0 = lds_ld4(xs_r), x1 = lds_ld4(xs_r + 64);
  v4f acc = mfma(wqf[0], x0, z4); acc = mfma(wqf[1], x1, acc);
  if ((l & 15) == 0) ws_st4(rs, q_st, lay, tag4(acc, tag));
  if (stat) {
    v4f sxx = mfma(x0, x0, z4); sxx = mfma(x1, x1, sxx);
    v4f sx = mfma(ones, x0, z4); sx = mfma(ones, x1, sx);
    const float h0 = u2f(f2u(sx.x) & 0xffffff00u), h1 = u2f(f2u(sxx.x) & 0xffffff00u);
    const v4f st = {h0, sx.x - h0, h1, sxx.x - h1};
    if (l == 0) ws_st4(rs, 0, lay + stat_off, tag4(st, tag));
  }
}

DEVI void qk_softmax(const v4u (&qx)[2], float cq, const v4u (&kf)[8], unsigned q_lw, unsigned qa, unsigned p_w, int l, int w) {
  const int lg = l >> 4, ln = l & 15;
  const v4f z4 = {0.f, 0.f, 0.f, 0.f};
  v4f a4 = asf(qx[0]); if (lg < 3) a4 += asf(qx[1]);
  const float U = tred4(a4);
  float sx = (ln < 7) ? u2f(qx[1].x) + u2f(qx[1].y) : 0.f, sxx = (ln < 7) ? u2f(qx[1].z) + u2f(qx[1].w) : 0.f;
  sx += dpp<ROR4>(sx); sxx += dpp<ROR4>(sxx); sx += dpp<ROR2>(sx); sxx += dpp<ROR2>(sxx); sx += dpp<ROR1>(sx); sxx += dpp<ROR1>(sxx);
  sx += dpp<ROR8>(sx); sxx += dpp<ROR8>(sxx);
  sx = rdlane(sx, 48); sxx = rdlane(sxx, 48);
  float m0, rden; ln_scalars(sx, sxx, m0, rden);
  const float u = U - m0 * cq;
  *(LAS u16*)q_lw = (u16)pk_bf16(u, u);
  BAR();
  v4f d0 = z4, d1 = z4;
#pragma unroll
  for (int s = 0; s < 4; ++s) { const v4u qf = lds_ld4(qa + s * 128); d0 = mfma(kf[s], qf, d0); d1 = mfma(kf[4 + s], qf, d1); }
  float t[4];
#pragma unroll
  for (int r = 0; r < 4; ++r) {
    auto sw = __builtin_amdgcn_permlane32_swap(f2u(d0[r]), f2u(d1[r]), false, false);
    const float s = u2f(sw[0]) + dpp<QP_X2>(u2f(sw[1]));
    t[r] = rbf(s * rden) * (1.4426950408889634f / 16.0f);
  }
  const float m = rows_max(fmaxf(fmaxf(t[0], t[1]), fmaxf(t[2], t[3])));
  float pr[4];
#pragma unroll
  for (int r = 0; r < 4; ++r) pr[r] = __builtin_amdgcn_exp2f(t[r] - m);
  const float ls = rows_sum((pr[0] + pr[1]) + (pr[2] + pr[3]));
  if (ln < 2) {
    v2u pp = {pk_bf16(pr[0], pr[1]), pk_bf16(pr[2], pr[3])};
    *(LAS v2u*)p_w = pp;
    if (lg == 0) { v2f st = {m, ls}; *(LAS v2f*)(L_S + (2 * w + ln) * 8) = st; }
  }
}

__global__ void __launch_bounds__(512, 2) fused_decode(Params p) {
  extern __shared__ __attribute__((aligned(16))) unsigned char smem[];
  const int tid = threadIdx.x, l = tid & 63, lg = l >> 4, ln = l & 15;
  const int w = __builtin_amdgcn_readfirstlane(tid >> 6);
  const int wg = blockIdx.x, g = wg & 7, j = wg >> 3;
  const bool isQ = j < 28, isO = j < 14, isF = j >= 14 && j < 28;
  const int qi = j % 7, qsub = j / 7;
  const int f = 14 * g + (j - 14);
  const rsrc_t rs = __builtin_amdgcn_make_buffer_rsrc(p.ws, 0, (int)(8u << 20), 0x00020000);
  STAMP(0);

  unsigned epoch, xq_raw, xr_raw, pf0 = 0, pf1 = 0;
  const unsigned c4 = (unsigned)(4 * ln + lg);
  {
    unsigned z = 0;
    asm volatile("global_load_dword %0, %1, %2 sc1" : "=v"(epoch) : "v"(z), "s"(p.ws) : "memory");
    const unsigned o1 = (64u * qi + c4) * 2u, o2 = (64u * (w < 7 ? w : 0) + c4) * 2u;
    asm volatile("global_load_ushort %0, %1, %2" : "=v"(xq_raw) : "v"(o1), "s"(p.x) : "memory");
    asm volatile("global_load_ushort %0, %1, %2" : "=v"(xr_raw) : "v"(o2), "s"(p.x) : "memory");
  }
  if (isQ) {
    const u16* wqb = p.wq + (size_t)(64 * qi + 8 * w) * 4096 + 512 * g + 128 * qsub;
    const unsigned vo = (unsigned)lg * 8192u + (unsigned)ln * 16u;
    dma16(wqb, vo, L_WQ + (2 * w) * 1024);
    dma16(wqb + 4 * 4096, vo, L_WQ + (2 * w + 1) * 1024);
  } else if (w == 0) {
    const u64 pc = __builtin_amdgcn_s_getpc();
    const unsigned vo = (unsigned)l * 128u;
    asm volatile("global_load_dword %0, %2, %3\n\tglobal_load_dword %1, %2, %3 offset:64" : "=&v"(pf0), "=&v"(pf1) : "v"(vo), "s"(pc) : "memory");
  }
  const size_t kvrow = (size_t)g * SK + (SK - WIN) + 128 * j + 16 * w;
  const u16* kb = p.kc + kvrow * 256 + 1024;
  const u16* vb = p.vc + kvrow * 256;
  const unsigned koff = (unsigned)(l & 7) * 512u + (unsigned)(l >> 3) * 16u;
  v4u kf[8];
  ldk<-2048>(kf[0], kb, koff); ldk<-2048 + 128>(kf[1], kb, koff); ldk<-2048 + 256>(kf[2], kb, koff);
  if (tid < 4) ((LAS unsigned*)L_ZERO)[tid] = 0u;

  const unsigned trl = (unsigned)(ln >> 2), trp = (unsigned)(ln & 3) * 8u;
  unsigned xs_w = L_XS + w * 128 + c4 * 2, xs_r = L_XS + w * 128 + lg * 16;
  unsigned q_st = WS_QREC + (unsigned)(g * 14336 + qi * 2048 + (128 * qsub + 16 * w + 4 * lg) * 4);
  unsigned qo0 = WS_QREC + (unsigned)(g * 14336 + lg * 2048 + (64 * w + 4 * ln) * 4);
  unsigned qo1 = lg < 3 ? qo0 + 8192u : WS_QSTAT + (unsigned)(g * 7 + (ln < 7 ? ln : 6)) * 128u;
  unsigned q_lw = L_Q + (w >> 2) * 640 + ((w & 3) * 64 + c4) * 2;
  unsigned qa = L_Q + (ln & 1) * 640 + lg * 32 + ((ln >> 1) & 1) * 16;
  unsigned p_w = L_P + ln * 256 + (16 * w + 4 * lg) * 2;
  unsigned pa[4];
  {
    const bool act = ((ln >> 1) & 1) == (lg >> 1);
    const unsigned a = L_P + (ln & 1) * 256 + ((ln >> 2) * 32 + 8 * lg) * 2;
#pragma unroll
    for (int tau = 0; tau < 4; ++tau) pa[tau] = (act && (ln >> 2) == tau) ? a : L_ZERO;
  }
  unsigned s_r = L_S + ln * 8;
  unsigned o_st = WS_OREC + (unsigned)((g * 32 + j) * 2048 + (ln & 1) * 1024 + (32 * w + 16 * (ln >> 1) + 4 * lg) * 4);
  unsigned x1o = WS_X1 + (64u * qi + c4) * 4u;
  unsigned dofs = WS_DREC + (unsigned)((16 * w + lg) * 1792 + (64 * qi + 4 * ln) * 4);
  unsigned red_a = L_RED + l * 4;
  unsigned r0, r1, r2, r3, r4;
  if (isO) {
    r0 = WS_OREC + (unsigned)((g * 32 + 4 * w) * 2048 + qsub * 1024 + l * 16);
    r1 = WS_OSTAT + (unsigned)((g * 32 + 4 * w + (l & 3)) * 128 + qsub * 16);
    r2 = L_OP + w * 528 + l * 8;
    r3 = L_OP + (ln & 7) * 528 + lg * 16;
    r4 = WS_XREC + (unsigned)((2 * g + qsub) * 1792 + (64 * qi + 16 * w + 4 * lg) * 4);
  } else {
    r0 = WS_XREC + (unsigned)(lg * 1792 + (64 * w + 4 * ln) * 4);
    r1 = (64u * w + c4) * 4u;
    r2 = L_G + w * 144 + ln * 64 + lg * 16;
    r3 = L_G + (lg < 2 ? l * 4 : 128 + (lg - 2) * 4);
    r4 = WS_DREC + (unsigned)(f * 1792 + (64 * w + l) * 4);
  }
  PIN(xs_w); PIN(xs_r); PIN(q_st); PIN(qo0); PIN(qo1); PIN(q_lw); PIN(qa); PIN(p_w); PIN(pa[0]); PIN(pa[1]); PIN(pa[2]); PIN(pa[3]);
  PIN(s_r); PIN(o_st); PIN(x1o); PIN(dofs); PIN(red_a); PIN(r0); PIN(r1); PIN(r2); PIN(r3); PIN(r4);

  asm volatile("s_waitcnt vmcnt(3)" : "+v"(epoch), "+v"(xq_raw), "+v"(xr_raw), "+v"(pf0), "+v"(pf1) :: "memory");
  BAR();
  const unsigned ep1 = (unsigned)__builtin_amdgcn_readfirstlane(epoch) + 1u, tag = ep1 & 0xffu;
  const v4u ones = {0x3f803f80u, 0x3f803f80u, 0x3f803f80u, 0x3f803f80u};
  const v4f z4 = {0.f, 0.f, 0.f, 0.f};
  const bool qstat = qsub == 0 && w == 0;
  const unsigned qstat_off = WS_QSTAT + (unsigned)(g * 7 + qi) * 128u;
  v4u wqf[2];
  {
    const unsigned a = L_WQ + (8 * lg + trl) * 256 + w * 32 + trp;
    wqf[0] = frag(a, a + 4 * 256); wqf[1] = frag(a + 32 * 256, a + 36 * 256);
  }
  STAMP(1);
  if (isQ) {
    q_stage(rs, 0, tag, u2f(xq_raw << 16), wqf, xs_w, xs_r, q_st, qstat, qstat_off, l);
    v4f cs = mfma(wqf[0], ones, z4); cs = mfma(wqf[1], ones, cs);
    if (ln == 0) ws_st4(rs, q_st, WS_CDELTA, tag4(cs, tag));
  }
  STAMP(2);
  float xres = u2f(xr_raw << 16);
  float cq, cgu = 0.f;
  v4u vf[8], w2f[8];
  {
    ldk<-2048 + 384>(kf[3], kb, koff);
    ldk<2048>(kf[4], kb, koff); ldk<2048 + 128>(kf[5], kb, koff); ldk<2048 + 256>(kf[6], kb, koff); ldk<2048 + 384>(kf[7], kb, koff);
    const unsigned v0 = ((unsigned)(l >> 5) * 512u + (unsigned)(l & 31) * 16u) ^ ((unsigned)(l >> 5) << 5);
    const unsigned lv = L_V + w * 8192;
    dma16x4(vb, v0, v0 ^ 64u, lv);
    v4u qx[2], cx[2];
    const unsigned co1 = lg < 3 ? qo1 : qo0;
    ld_sc1_x4(qx[0], p.ws, qo0); ld_sc1_x4(qx[1], p.ws, qo1); ld_sc1_x4(cx[0], p.ws + WS_CDELTA, qo0); ld_sc1_x4(cx[1], p.ws + WS_CDELTA, co1);
    dma16x4(vb + 2048, v0 ^ 128u, v0 ^ 192u, lv + 4096);
    asm volatile("s_waitcnt vmcnt(4)" : "+v"(qx[0]), "+v"(qx[1]), "+v"(cx[0]), "+v"(cx[1]), "+v"(kf[0]), "+v"(kf[1]), "+v"(kf[2]), "+v"(kf[3]),
                 "+v"(kf[4]), "+v"(kf[5]), "+v"(kf[6]), "+v"(kf[7]) :: "memory");
    if (!allok(bad4(qx[0], tag) | bad4(qx[1], tag) | bad4(cx[0], tag) | bad4(cx[1], tag))) {
      poll_2x4(rs, qo0, qo1, 0, tag, qx); poll_2x4(rs, qo0, co1, WS_CDELTA, tag, cx);
    }
    STAMP(3);
    v4f c4v = asf(cx[0]); if (lg < 3) c4v += asf(cx[1]);
    cq = tred4(c4v);
    qk_softmax(qx, cq, kf, q_lw, qa, p_w, l, w);
    STAMP(4);
    if (isO) {
      const u16* wob = p.wo + (size_t)(256 * (2 * g + qsub) + 32 * w) * D + 64 * qi;
      const unsigned vo = (unsigned)(l >> 3) * 896u + (unsigned)(l & 7) * 16u;
#pragma unroll
      for (int u = 0; u < 4; ++u) dma16(wob + u * 8 * D, vo, L_W2 + (4 * w + u) * 1024);
      VMWAIT(4);
    } else {
      VMWAIT(0);
    }
    BAR();
    const unsigned h = trl | ((unsigned)(lg & 1) << 2);
#pragma unroll
    for (int t = 0; t < 2; ++t) {
      const unsigned a = L_V + (8 * lg + trl) * 512 + (((unsigned)(2 * w + t) ^ h) * 32) + trp;
#pragma unroll
      for (int tau = 0; tau < 4; ++tau) vf[t * 4 + tau] = frag(a + tau * 16384, a + tau * 16384 + 2048);
    }
  }

  for (int L = 0;; ++L) {
    const unsigned lay = (unsigned)L * WS_L;
    STAMP(5 + 10 * L);
    {
      const v2f st = *(LAS v2f*)s_r;
      v4f o0 = z4, o1 = z4;
#pragma unroll
      for (int tau = 0; tau < 4; ++tau) {
        const v4u pf = lds_ld4(pa[tau]);
        o0 = mfma(vf[tau], pf, o0); o1 = mfma(vf[4 + tau], pf, o1);
      }
      float M = st.x; M = fmaxf(M, dpp<ROR2>(M)); M = fmaxf(M, dpp<ROR4>(M)); M = fmaxf(M, dpp<ROR8>(M));
      M = u2f(f2u(M) & 0xffffff00u);
      const float fs = __builtin_amdgcn_exp2f(st.x - M);
      float ls = fs * st.y; ls += dpp<ROR2>(ls); ls += dpp<ROR4>(ls); ls += dpp<ROR8>(ls);
      v4f o;
#pragma unroll
      for (int r = 0; r < 4; ++r) {
        float a = o0[r] * fs, b = o1[r] * fs;
        a += dpp<ROR2>(a); b += dpp<ROR2>(b); a += dpp<ROR4>(a); b += dpp<ROR4>(b); a += dpp<ROR8>(a); b += dpp<ROR8>(b);
        o[r] = (ln & 2) ? b : a;
      }
      if (ln < 4) ws_st4(rs, o_st, lay, tag4(o, tag));
      if (w == 0 && l < 2) { v4u sv = {tagv(M, tag), tagv(ls, tag), ep1, ep1}; ws_st4(rs, (unsigned)l * 16u, lay + WS_OSTAT + (unsigned)(g * 32 + j) * 128u, sv); }
    }
    STAMP(6 + 10 * L);
    if (isO) {
      if (L == 0) {
        VMWAIT(0); BAR();
        const unsigned a = L_W2 + (8 * lg + trl) * 128 + (w & 3) * 32 + trp;
#pragma unroll
        for (int s = 0; s < 8; ++s) w2f[s] = frag(a + s * 4096, a + s * 4096 + 512);
      }
      v4u ox[4], os;
      for (unsigned sp = 0; sp < SPIN_CAP; ++sp) {
        asm volatile("" ::: "memory");
        os = ws_ld4(rs, r1, lay);
#pragma unroll
        for (int k = 0; k < 4; ++k) ox[k] = ws_ld4(rs, r0, lay + k * 2048);
        const unsigned bad = bad4(ox[0], tag) | bad4(ox[1], tag) | bad4(ox[2], tag) | bad4(ox[3], tag) | bad4(os, tag);
        if (__all((bad & 0xffu) == 0u && os.z == ep1)) break;
      }
      STAMP(7 + 10 * L);
      const float Ms = u2f(os.x & 0xffffff00u), lsv = u2f(os.y);
      float m = fmaxf(Ms, dpp<QP_X1>(Ms)); m = fmaxf(m, dpp<QP_X2>(m));
      const float e = __builtin_amdgcn_exp2f(Ms - m);
      float ls = e * lsv; ls += dpp<QP_X1>(ls); ls += dpp<QP_X2>(ls);
      const v4f P = asf(ox[0]) * dpp<0x00>(e) + asf(ox[1]) * dpp<0x55>(e) + asf(ox[2]) * dpp<0xAA>(e) + asf(ox[3]) * dpp<0xFF>(e);
      const v2u pp = {pk_bf16(P.x, P.y), pk_bf16(P.z, P.w)};
      *(LAS v2u*)r2 = pp;
      if (l == 0) { v2f st = {m, ls}; *(LAS v2f*)(L_OS + w * 8) = st; }
      BAR();
      if (w < 4) {
        const v2f st = *(LAS v2f*)(L_OS + (ln & 7) * 8);
        v4f acc = z4;
#pragma unroll
        for (int s = 0; s < 8; ++s) acc = mfma(w2f[s], lds_ld4(r3 + s * 64), acc);
        float M = st.x; M = fmaxf(M, dpp<ROR1>(M)); M = fmaxf(M, dpp<ROR2>(M)); M = fmaxf(M, dpp<ROR4>(M));
        const float fs = __builtin_amdgcn_exp2f(st.x - M);
        float Ls = fs * st.y; Ls += dpp<ROR1>(Ls); Ls += dpp<ROR2>(Ls); Ls += dpp<ROR4>(Ls);
        const float sc = fs * __builtin_amdgcn_rcpf(Ls);
        v4f y;
#pragma unroll
        for (int r = 0; r < 4; ++r) { float a = acc[r] * sc; a += dpp<ROR1>(a); a += dpp<ROR2>(a); a += dpp<ROR4>(a); y[r] = a; }
        if (ln == 0) ws_st4(rs, r4, lay, tag4(y, tag));
      }
      STAMP(8 + 10 * L);
    } else if (isF) {
      if (L == 0) {
        const unsigned vo = (unsigned)(l >> 1) * 3584u + (unsigned)(l & 1) * 16u;
        const int wc = w < 7 ? w : 6;
        dma16(p.wg + (size_t)(64 * wc) * FF + 16 * f, vo, L_W2 + wc * 2048);
        dma16(p.wg + (size_t)(64 * wc + 32) * FF + 16 * f, vo, L_W2 + wc * 2048 + 1024);
        dma16(p.wu + (size_t)(64 * wc) * FF + 16 * f, vo, L_W2 + 14336 + wc * 2048);
        dma16(p.wu + (size_t)(64 * wc + 32) * FF + 16 * f, vo, L_W2 + 14336 + wc * 2048 + 1024);
        dma16(p.wd + (size_t)(16 * f) * D + wc * 1024, (unsigned)l * 16u, L_W2 + 28672 + wc * 2048);
        dma16(p.wd + (size_t)(16 * f) * D + wc * 1024 + 512, (unsigned)l * 16u, L_W2 + 28672 + wc * 2048 + 1024);
        VMWAIT(0); BAR();
#pragma unroll
        for (int s = 0; s < 2; ++s) {
          const unsigned a = L_W2 + (64 * wc + 32 * s + 8 * lg + trl) * 32 + trp;
          w2f[s] = frag(a, a + 128); w2f[2 + s] = frag(a + 14336, a + 14336 + 128);
        }
        const unsigned a = L_W2 + 28672 + trl * 896 + (64 * wc + 16 * lg) * 2 + trp;
        w2f[4] = frag(a, a + 4 * 896); w2f[5] = frag(a + 8 * 896, a + 12 * 896);
        v4f cg = mfma(w2f[0], ones, z4); cg = mfma(w2f[1], ones, cg);
        v4f cu = mfma(w2f[2], ones, z4); cu = mfma(w2f[3], ones, cu);
        if (w < 7 && ln < 2) *(LAS v4f*)r2 = ln ? cu : cg;
        BAR();
#pragma unroll
        for (int b = 0; b < 7; ++b) cgu += lds_ldf(r3 + b * 144);
        BAR();
      } else {
        if (w < 7) xres = u2f(poll1(rs, r1, WS_X2, tag) & 0xffff0000u);
      }
      if (w < 7) {
        v4u xx[4];
        poll_4x4<4 * 1792>(rs, r0, lay, tag, xx);
        STAMP(7 + 10 * L);
        const float att = tred4((asf(xx[0]) + asf(xx[1])) + (asf(xx[2]) + asf(xx[3])));
        const float x1 = rbf(xres + rbf(att));
        *(LAS u16*)xs_w = (u16)(f2u(x1) >> 16);
        if (f == 0) ws_st1(rs, r1, lay + WS_X1, f2u(x1) | tag);
        LGKM0();
        const v4u x0 = lds_ld4(xs_r), x1f = lds_ld4(xs_r + 64);
        v4f ga = mfma(w2f[0], x0, z4); v4f ua = mfma(w2f[2], x0, z4);
        v4f sxx = mfma(x0, x0, z4); v4f sx = mfma(ones, x0, z4);
        ga = mfma(w2f[1], x1f, ga); ua = mfma(w2f[3], x1f, ua);
        sxx = mfma(x1f, x1f, sxx); sx = mfma(ones, x1f, sx);
        if (ln < 2) *(LAS v4f*)r2 = ln ? ua : ga;
        if (l == 2) { v2f st = {sx.x, sxx.x}; *(LAS v2f*)(L_G + w * 144 + 128) = st; }
      }
      BAR();
      if (w < 7) {
        float s = 0.f;
#pragma unroll
        for (int b = 0; b < 7; ++b) s += lds_ldf(r3 + b * 144);
        float m, rd; ln_scalars(rdlane(s, 32), rdlane(s, 48), m, rd);
        const float val = rbf((s - m * cgu) * rd);
        auto sw = __builtin_amdgcn_permlane16_swap(f2u(val), f2u(val), false, false);
        const float gg = u2f(sw[0]), uu = u2f(sw[1]);
        const float sg = rbf(gg * __builtin_amdgcn_rcpf(1.0f + __builtin_amdgcn_exp2f(gg * -1.4426950408889634f)));
        const float h = sg * uu;
        const unsigned hp = pk_bf16(h, dpp<QP_X1>(h));
        float d0 = 0.f, d1 = 0.f;
#pragma unroll
        for (int k = 0; k < 4; ++k) {
          d0 = dot2(w2f[4][k], (unsigned)__builtin_amdgcn_readlane((int)hp, 2 * k), d0);
          d1 = dot2(w2f[5][k], (unsigned)__builtin_amdgcn_readlane((int)hp, 8 + 2 * k), d1);
        }
        ws_st1(rs, r4, lay, tagv(d0 + d1, tag));
      }
      STAMP(8 + 10 * L);
    }
    const bool doX = (L == 0) ? isQ : (g == 0 && j < 7);
    if (doX) {
      const float x1 = u2f(poll1(rs, x1o, lay, tag) & 0xffff0000u);
      if (w < 7) {
        v4u dx[4];
        poll_4x4<4 * 1792>(rs, dofs, lay, tag, dx);
        *(LAS float*)(red_a + w * 256) = tred4((asf(dx[0]) + asf(dx[1])) + (asf(dx[2]) + asf(dx[3])));
      }
      STAMP(9 + 10 * L);
      BAR();
      float s = 0.f;
#pragma unroll
      for (int b = 0; b < 7; ++b) s += lds_ldf(red_a + b * 256);
      const float xv = rbf(x1 + rbf(s));
      if (L == 1) {
        if (w == 0) {
          p.out[64 * qi + c4] = (u16)(f2u(xv) >> 16);
          if (j == 0 && l == 0) ws_st1(rs, 0, WS_EPOCH, ep1);
        }
        STAMP(20);
        break;
      }
      q_stage(rs, WS_L, tag, xv, wqf, xs_w, xs_r, q_st, qstat, qstat_off, l);
      if (g == 0 && qsub == 0 && w == 1) ws_st1(rs, x1o, WS_X2 - WS_X1, f2u(xv) | tag);
    }
    if (L == 1) break;
    STAMP(12);
    v4u qx[2];
    poll_2x4(rs, qo0, qo1, WS_L, tag, qx);
    STAMP(13);
    qk_softmax(qx, cq, kf, q_lw, qa, p_w, l, w);
    STAMP(14);
    BAR();
  }
}
}

extern "C" void kernel_launch(void* const* d_in, const int* in_sizes, int n_in, void* d_out, int out_size, void* d_ws, size_t ws_size,
                              hipStream_t stream) {
  Params p;
  p.x = (const u16*)d_in[0]; p.kc = (const u16*)d_in[1]; p.vc = (const u16*)d_in[2]; p.wq = (const u16*)d_in[3]; p.wo = (const u16*)d_in[4];
  p.wg = (const u16*)d_in[6]; p.wu = (const u16*)d_in[7]; p.wd = (const u16*)d_in[8];
  p.out = (u16*)d_out; p.ws = (unsigned char*)d_ws;
  (void)hipFuncSetAttribute((const void*)fused_decode, hipFuncAttributeMaxDynamicSharedMemorySize, (int)LDS_BYTES);
  hipLaunchKernelGGL(fused_decode, dim3(256), dim3(512), LDS_BYTES, stream, p);
}
```

```cpp
#include <hip/hip_runtime.h>
#include <stdint.h>
#pragma clang diagnostic ignored "-Wint-to-pointer-cast"

typedef unsigned short u16;
typedef unsigned long long u64;
typedef unsigned v4u __attribute__((ext_vector_type(4)));
typedef unsigned v2u __attribute__((ext_vector_type(2)));
typedef float v4f __attribute__((ext_vector_type(4)));
typedef float v2f __attribute__((ext_vector_type(2)));
typedef short v4s __attribute__((ext_vector_type(4)));
typedef __bf16 v8bf __attribute__((ext_vector_type(8)));
typedef __bf16 v2bf __attribute__((ext_vector_type(2)));
#define LAS __attribute__((address_space(3)))
#define GAS __attribute__((address_space(1)))
#define DEVI __device__ __forceinline__

#define STAMPS 0
#define SHIFT_Q 1
#define SHIFT_O 1
#define D_O 0
#define D_X1 0
#define D_D 4
#define D_Q 10
#define D_QH 14
#define D_QS 40
#define RB0 1
#define RB1 0
#define V_DELAY 4
#define K_EARLY 6
#define W2_EARLY 2

namespace {
constexpr int D = 448, SK = 65536, WIN = 4096, FF = 1792;
constexpr unsigned WS_EPOCH = 0;
constexpr unsigned WS_QREC = 4096;
constexpr unsigned WS_QSTAT = WS_QREC + 8 * 7 * 2048;
constexpr unsigned WS_OREC = WS_QSTAT + 8 * 7 * 128;
constexpr unsigned WS_OSTAT = WS_OREC + 256 * 2048;
constexpr unsigned WS_XREC = WS_OSTAT + 256 * 128;
constexpr unsigned WS_X1 = WS_XREC + 16 * 1792;
constexpr unsigned WS_X2 = WS_X1 + 1792;
constexpr unsigned WS_XFLAG = WS_X2 + 1792;
constexpr unsigned WS_DREC = WS_XFLAG + 16 * 128;
constexpr unsigned WS_L = 1u << 20;
static_assert(WS_DREC + 112 * 1792 <= WS_L, "layer block");
constexpr unsigned WS_CDELTA = 2 * WS_L;
constexpr unsigned WS_STAMP = 4u << 20;
constexpr unsigned L_V = 0;
constexpr unsigned L_WQ = 65536;
constexpr unsigned L_W2 = 81920;
constexpr unsigned L_Q = L_W2 + 43008;
constexpr unsigned L_P = L_Q + 1280;
constexpr unsigned L_S = L_P + 4224;
constexpr unsigned L_OP = L_S + 64;
constexpr unsigned L_OS = L_OP + 8 * 528;
constexpr unsigned L_XS = L_OS + 64;
constexpr unsigned L_RED = L_XS + 1024;
constexpr unsigned L_G = L_RED + 1792;
constexpr unsigned L_END = L_G + 7 * 144;
constexpr unsigned L_STAMP = (L_END + 15) / 16 * 16;
constexpr unsigned LDS_BYTES = L_STAMP + 512;
constexpr unsigned SPIN_CAP = 1u << 16;
constexpr unsigned CODE_PF_BYTES = 256 * 44;

struct Params { const u16 *x, *kc, *vc, *wq, *wo, *wg, *wu, *wd; u16* out; unsigned char* ws; };
typedef __amdgpu_buffer_rsrc_t rsrc_t;

DEVI float u2f(unsigned u) { return __builtin_bit_cast(float, u); }
DEVI unsigned f2u(float f) { return __builtin_bit_cast(unsigned, f); }
DEVI unsigned pk_bf16(float lo, float hi) { v2f f = {lo, hi}; return __builtin_bit_cast(unsigned, __builtin_convertvector(f, v2bf)); }
DEVI float rbf(float f) { return u2f(pk_bf16(0.f, f)); }
DEVI v4f mfma(v4u a, v4u b, v4f c) { return __builtin_amdgcn_mfma_f32_16x16x32_bf16(__builtin_bit_cast(v8bf, a), __builtin_bit_cast(v8bf, b), c, 0, 0, 0); }
DEVI float dot2(unsigned a, unsigned b, float c) { return __builtin_amdgcn_fdot2_f32_bf16(__builtin_bit_cast(v2bf, a), __builtin_bit_cast(v2bf, b), c, false); }
template <int CTRL> DEVI float dpp(float x) { return u2f((unsigned)__builtin_amdgcn_mov_dpp((int)f2u(x), CTRL, 0xf, 0xf, true)); }
constexpr int ROR1 = 0x121, ROR2 = 0x122, ROR4 = 0x124, ROR8 = 0x128, SHL4 = 0x104, QP_X1 = 0xB1, QP_X2 = 0x4E;
DEVI float fmx(float a, float b) { return __builtin_amdgcn_fmed3f(a, b, __builtin_inff()); }
DEVI float red32(float a, float b) { auto r = __builtin_amdgcn_permlane32_swap(f2u(a), f2u(b), false, false); return u2f(r[0]) + u2f(r[1]); }
DEVI float red16(float a, float b) { auto r = __builtin_amdgcn_permlane16_swap(f2u(a), f2u(b), false, false); return u2f(r[0]) + u2f(r[1]); }
DEVI float tred4(v4f a) { return red16(red32(a.x, a.z), red32(a.y, a.w)); }
DEVI float rows_max(float x) {
  auto r = __builtin_amdgcn_permlane32_swap(f2u(x), f2u(x), false, false); x = fmx(u2f(r[0]), u2f(r[1]));
  auto s = __builtin_amdgcn_permlane16_swap(f2u(x), f2u(x), false, false); return fmx(u2f(s[0]), u2f(s[1]));
}
DEVI float rows_sum(float x) { x = red32(x, x); return red16(x, x); }
DEVI float row_sum(float x) { x += dpp<ROR8>(x); x += dpp<ROR4>(x); x += dpp<ROR2>(x); x += dpp<ROR1>(x); return x; }
DEVI float rdlane(float x, int lane) { return u2f((unsigned)__builtin_amdgcn_readlane((int)f2u(x), lane)); }
DEVI unsigned tagv(float v, unsigned tag) { return (f2u(v) & 0xffffff00u) | tag; }
DEVI v4u tag4(v4f v, unsigned tag) { v4u r = {tagv(v.x, tag), tagv(v.y, tag), tagv(v.z, tag), tagv(v.w, tag)}; return r; }
DEVI v4f untag4(v4u x, unsigned tag, unsigned& bad) { x ^= tag; bad |= (x.x | x.y) | (x.z | x.w); return __builtin_bit_cast(v4f, x); }
#define BAR() asm volatile("s_waitcnt lgkmcnt(0)\n\ts_barrier" ::: "memory")
#define LGKM0() asm volatile("s_waitcnt lgkmcnt(0)" ::: "memory")
#define VMWAIT(n) asm volatile("s_waitcnt vmcnt(%0)" :: "i"(n) : "memory")
#define PIN(x) asm volatile("" : "+v"(x))
#define UNDEF4(x) asm volatile("" : "=v"(x))
template <int N> DEVI void nap() { if (N > 0) __builtin_amdgcn_s_sleep(N); }

DEVI void dma16(const void* sbase, unsigned voff, unsigned ldsaddr) {
  unsigned keep;
  asm volatile("s_mov_b32 %0, m0\n\ts_mov_b32 m0, %3\n\ts_nop 3\n\tglobal_load_lds_dwordx4 %1, %2 nt\n\ts_mov_b32 m0, %0"
               : "=&s"(keep) : "v"(voff), "s"(sbase), "s"(ldsaddr) : "memory");
}
DEVI void dma16x4(const void* sbase, unsigned va, unsigned vb, unsigned vc, unsigned vd, unsigned ldsaddr) {
  unsigned keep;
  asm volatile("s_mov_b32 %0, m0\n\ts_mov_b32 m0, %6\n\ts_nop 3\n\t"
               "global_load_lds_dwordx4 %1, %5 nt\n\tglobal_load_lds_dwordx4 %2, %5 offset:1024 nt\n\t"
               "global_load_lds_dwordx4 %3, %5 offset:2048 nt\n\tglobal_load_lds_dwordx4 %4, %5 offset:3072 nt\n\ts_mov_b32 m0, %0"
               : "=&s"(keep) : "v"(va), "v"(vb), "v"(vc), "v"(vd), "s"(sbase), "s"(ldsaddr) : "memory");
}
template <int OFF> DEVI void ldk(v4u& dst, const void* sbase, unsigned voff) {
  asm volatile("global_load_dwordx4 %0, %1, %2 offset:%3 nt" : "=v"(dst) : "v"(voff), "s"(sbase), "i"(OFF) : "memory");
}
DEVI v4u ws_ld4(rsrc_t rs, unsigned vo, unsigned so) { return __builtin_amdgcn_raw_buffer_load_b128(rs, vo, so, 16); }
DEVI unsigned ws_ld1(rsrc_t rs, unsigned vo, unsigned so) { return __builtin_amdgcn_raw_buffer_load_b32(rs, vo, so, 16); }
DEVI void ws_st4(rsrc_t rs, unsigned vo, unsigned so, v4u v) { __builtin_amdgcn_raw_buffer_store_b128(v, rs, vo, so, 16); }
DEVI void ws_st1(rsrc_t rs, unsigned vo, unsigned so, unsigned v) { __builtin_amdgcn_raw_buffer_store_b32(v, rs, vo, so, 16); }
DEVI v4u lds_ld4(unsigned a) { return *(LAS v4u*)a; }
DEVI float lds_ldf(unsigned a) { return *(LAS float*)a; }
DEVI v2u tr_read(unsigned a) { return __builtin_bit_cast(v2u, __builtin_amdgcn_ds_read_tr16_b64_v4i16((LAS v4s*)a)); }
DEVI v4u frag(unsigned a0, unsigned a1) { v2u lo = tr_read(a0), hi = tr_read(a1); v4u r = {lo.x, lo.y, hi.x, hi.y}; return r; }
DEVI bool allok(unsigned bad) { return __all((bad & 0xffu) == 0u); }

template <unsigned STR> DEVI void poll_4x4(rsrc_t rs, unsigned vo, unsigned so, unsigned tag, v4f (&y)[4]) {
  for (unsigned sp = 0; sp < SPIN_CAP; ++sp) {
    asm volatile("" ::: "memory");
    v4u x[4]; unsigned bad = 0;
#pragma unroll
    for (int k = 0; k < 4; ++k) x[k] = ws_ld4(rs, vo, so + k * STR);
#pragma unroll
    for (int k = 0; k < 4; ++k) y[k] = untag4(x[k], tag, bad);
    if (allok(bad)) break;
    nap<2>();
  }
}
DEVI void poll_2x4(rsrc_t rs, unsigned v0, unsigned v1, unsigned so, unsigned tag, v4f (&y)[2]) {
  for (unsigned sp = 0; sp < SPIN_CAP; ++sp) {
    asm volatile("" ::: "memory");
    unsigned bad = 0;
    const v4u x0 = ws_ld4(rs, v0, so), x1 = ws_ld4(rs, v1, so);
    y[0] = untag4(x0, tag, bad); y[1] = untag4(x1, tag, bad);
    if (allok(bad)) break;
    nap<2>();
  }
}
DEVI unsigned poll1(rsrc_t rs, unsigned vo, unsigned so, unsigned tag) {
  unsigned x = 0;
  for (unsigned sp = 0; sp < SPIN_CAP; ++sp) {
    asm volatile("" ::: "memory");
    x = ws_ld1(rs, vo, so) ^ tag;
    if (allok(x)) break;
  }
  return x;
}
DEVI void ln_scalars(float sx, float sxx, float& m, float& rden) {
  const float mean = sx * (1.0f / D);
  const float var = fmx(sxx * (1.0f / D) - mean * mean, 0.f);
  m = rbf(mean);
  const float den = rbf(__builtin_amdgcn_sqrtf(rbf(rbf(var) + 1e-5f)));
  rden = __builtin_amdgcn_rcpf(den);
}

#define STAMP(i) do {} while (0)

__global__ void __launch_bounds__(512, 2) fused_decode(Params p) {
  extern __shared__ __attribute__((aligned(16))) unsigned char smem[];
  const int tid = threadIdx.x, l = tid & 63, lg = l >> 4, ln = l & 15;
  const int w = __builtin_amdgcn_readfirstlane(tid >> 6);
  const int wg = blockIdx.x, g = wg & 7, j = wg >> 3;
  const int gq = (g + SHIFT_Q) & 7, go = (g + SHIFT_O) & 7;
  const bool isQ = j < 28, isO = j < 14, isF = j >= 14 && j < 28, lo = w < 4;
  const int qi = j % 7, qsub = j / 7;
  const int f = 14 * g + (j - 14);
  const rsrc_t rs = __builtin_amdgcn_make_buffer_rsrc(p.ws, 0, (int)(8u << 20), 0x00020000);
  STAMP(0);
  const unsigned c4 = (unsigned)(4 * ln + lg);
  const size_t kvrow = (size_t)g * SK + (SK - WIN) + 128 * j;
  const int rot = (w & 3) + j + 5 * g;
  const unsigned trl = (unsigned)(ln >> 2), trp = (unsigned)(ln & 3) * 8u;
  const v4u ones = {0x3f803f80u, 0x3f803f80u, 0x3f803f80u, 0x3f803f80u};
  const v4f z4 = {0.f, 0.f, 0.f, 0.f};

  unsigned epoch, xq_raw, xr_raw;
  v4u xall;
  v4u kf[16];
  const u16* kb0 = p.kc + kvrow * 256;
#define KB(T) (kb0 + ((4 * (T) + rot) & 15) * 2048)
  const unsigned koff = (unsigned)(l & 7) * 512u + (unsigned)(l >> 3) * 16u;
  {
    unsigned z = 0;
    asm volatile("global_load_dword %0, %1, %2 sc1" : "=v"(epoch) : "v"(z), "s"(p.ws) : "memory");
    const unsigned o1 = (64u * qi + c4) * 2u, o2 = (64u * (w < 7 ? w : 0) + c4) * 2u, o3 = (unsigned)(l < 56 ? l : 0) * 16u;
    asm volatile("global_load_ushort %0, %1, %2" : "=v"(xq_raw) : "v"(o1), "s"(p.x) : "memory");
    asm volatile("global_load_ushort %0, %1, %2" : "=v"(xr_raw) : "v"(o2), "s"(p.x) : "memory");
    asm volatile("global_load_dwordx4 %0, %1, %2" : "=v"(xall) : "v"(o3), "s"(p.x) : "memory");
  }
#define LDK(i) ldk<((i) & 3) * 128>(kf[i], KB((i) >> 2), koff)
  if (lo) {
    if (isQ) {
      const u16* wqb = p.wq + (size_t)(64 * qi) * 4096 + 512 * gq + 128 * qsub + 32 * w;
      const unsigned vo = (unsigned)(l >> 2) * 8192u + (unsigned)(l & 3) * 16u;
#pragma unroll
      for (int u = 0; u < 4; ++u) dma16(wqb + u * 16 * 4096, vo, L_WQ + w * 4096 + u * 1024);
    } else {
      const u64 pc = __builtin_amdgcn_s_getpc() & ~63ull;
      unsigned keep;
      asm volatile("s_mov_b32 %0, m0\n\ts_mov_b32 m0, %3\n\ts_nop 3\n\tglobal_load_lds_dword %1, %2\n\ts_mov_b32 m0, %0"
                   : "=&s"(keep) : "v"((unsigned)(w * 64 + l) * (CODE_PF_BYTES / 256u)), "s"(pc), "s"(L_WQ + w * 4096) : "memory");
    }
    LDK(0); LDK(1); LDK(2); LDK(3); LDK(4); LDK(5);
  } else {
    nap<V_DELAY>();
    const u16* vb = p.vc + kvrow * 256;
    const unsigned v0 = ((unsigned)(l >> 5) * 512u + (unsigned)(l & 31) * 16u) ^ ((unsigned)(l >> 5) << 5);
    const unsigned v1 = v0 ^ 64u, v2 = v0 ^ 128u, v3 = v0 ^ 192u, lv = L_V + (w & 3) * 16384;
#pragma unroll
    for (int u = 0; u < 4; ++u) dma16x4(vb + ((4 * u + rot) & 15) * 2048, v0, v1, v2, v3, lv + u * 4096);
#pragma unroll
    for (int i = 0; i < 6; ++i) UNDEF4(kf[i]);
  }
  if (tid < 264) *(LAS v4u*)(L_P + tid * 16) = v4u{0u, 0u, 0u, 0u};

  unsigned xs_w = L_XS + w * 128 + c4 * 2, xs_r = L_XS + w * 128 + lg * 16;
  unsigned q_st = WS_QREC + (unsigned)(gq * 14336 + qi * 2048 + (128 * qsub + 32 * w + 16 * ln + 4 * lg) * 4);
  unsigned qo0 = WS_QREC + (unsigned)(g * 14336 + lg * 2048 + (64 * w + 4 * ln) * 4);
  unsigned qo1 = lg < 3 ? qo0 + 8192u : WS_QSTAT + (unsigned)(g * 7 + (ln < 7 ? ln : 6)) * 128u;
  unsigned q_lw = L_Q + (w >> 2) * 640 + ((w & 3) * 64 + c4) * 2;
  unsigned qa = L_Q + (ln & 1) * 640 + lg * 32 + ((ln >> 1) & 1) * 16;
  unsigned p_w = L_P + ((w * 4 + lg) * 2 + ln) * 128 + 112;
  unsigned pa = L_P + (((ln >> 2) * 4 + lg) * 2 + (ln & 1)) * 128 + (7 - (ln >> 1)) * 16;
  unsigned s_r = L_S + (ln >> 2) * 16 + (ln & 1) * 8;
  unsigned o_st = WS_OREC + (unsigned)((g * 32 + j) * 2048 + (ln & 1) * 1024 + (64 * w + 32 * (ln >> 3) + 16 * ((ln >> 1) & 1) + 4 * lg) * 4);
  unsigned x1o = WS_X1 + (64u * qi + c4) * 4u;
  unsigned dofs = WS_DREC + (unsigned)((16 * w + lg) * 1792 + (64 * qi + 4 * ln) * 4);
  unsigned red_a = L_RED + l * 4;
  unsigned r0, r1, r2, r3, r4;
  if (isO) {
    r0 = WS_OREC + (unsigned)((go * 32 + 4 * w) * 2048 + qsub * 1024 + l * 16);
    r1 = WS_OSTAT + (unsigned)((go * 32 + 4 * w + (l & 3)) * 128 + qsub * 16);
    r2 = L_OP + w * 528 + l * 8;
    r3 = L_OP + (ln & 7) * 528 + lg * 16;
    r4 = WS_XREC + (unsigned)((2 * go + qsub) * 1792 + (64 * qi + 16 * w + 4 * lg) * 4);
  } else {
    r0 = WS_XREC + (unsigned)(lg * 1792 + (64 * w + 4 * ln) * 4);
    r1 = (64u * w + c4) * 4u;
    r2 = L_G + w * 144 + ln * 64 + lg * 16;
    r3 = L_G + (lg < 2 ? l * 4 : 128 + (lg - 2) * 4);
    r4 = WS_DREC + (unsigned)(f * 1792 + (64 * w + l) * 4);
  }
  PIN(xs_w); PIN(xs_r); PIN(q_st); PIN(qo0); PIN(qo1); PIN(q_lw); PIN(qa); PIN(p_w); PIN(pa);
  PIN(s_r); PIN(o_st); PIN(x1o); PIN(dofs); PIN(red_a); PIN(r0); PIN(r1); PIN(r2); PIN(r3); PIN(r4);

  if (lo) VMWAIT(6);
  asm volatile("s_waitcnt vmcnt(16)" : "+v"(epoch), "+v"(xq_raw), "+v"(xr_raw), "+v"(xall) :: "memory");
  const unsigned ep1 = (unsigned)__builtin_amdgcn_readfirstlane(epoch) + 1u, tag = ep1 & 0xffu;
  const unsigned qstat_off = WS_QSTAT + (unsigned)(gq * 7 + qi) * 128u;
  STAMP(1);
  float xres = u2f(xr_raw << 16);
  float cq = 0.f, cgu = 0.f;
  float xv = 0.f;
  v4u wqf[4], vf[16];
  if (lo) {
    float e[8];
#pragma unroll
    for (int k = 0; k < 4; ++k) { e[2 * k] = u2f(xall[k] << 16); e[2 * k + 1] = u2f(xall[k] & 0xffff0000u); }
    float s = ((e[0] + e[1]) + (e[2] + e[3])) + ((e[4] + e[5]) + (e[6] + e[7]));
    s = rows_sum(row_sum(l < 56 ? s : 0.f));
    const float mean = s * (1.0f / D);
    float s2 = 0.f;
#pragma unroll
    for (int k = 0; k < 8; ++k) { const float d = e[k] - mean; s2 += d * d; }
    s2 = rows_sum(row_sum(l < 56 ? s2 : 0.f));
    const float den = rbf(__builtin_amdgcn_sqrtf(rbf(rbf(s2 * (1.0f / D)) + 1e-5f)));
    xv = rbf(rbf(u2f(xq_raw << 16) - rbf(mean)) / den);
  }
  unsigned wq_a = L_WQ + (w & 3) * 4096 + (8 * lg + trl) * 64 + trp;
  unsigned w2_a = isO ? L_W2 + trp + (8 * lg + trl) * 128 + (w & 3) * 32 : L_W2 + trp + (64 * (w < 7 ? w : 6) + 8 * lg + trl) * 32;
  unsigned wd_a = L_W2 + trp + 28672 + trl * 896 + (64 * (w < 7 ? w : 6) + 16 * lg) * 2;
  PIN(wq_a); PIN(w2_a); PIN(wd_a);
#define READ_WQF() _Pragma("unroll") for (int t = 0; t < 2; ++t) _Pragma("unroll") for (int s_ = 0; s_ < 2; ++s_) wqf[2 * t + s_] = frag(wq_a + t * 32 + s_ * 2048, wq_a + t * 32 + s_ * 2048 + 256)
  READ_WQF();
#pragma unroll
  for (int i = 6; i < 16; ++i) UNDEF4(kf[i]);
#pragma unroll
  for (int i = 0; i < 16; ++i) UNDEF4(vf[i]);

  for (int L = 0;; ++L) {
    const unsigned lay = (unsigned)L * WS_L;
    if (isQ && lo) {
      *(LAS u16*)xs_w = (u16)(f2u(xv) >> 16);
      LGKM0();
      const v4u x0 = lds_ld4(xs_r), x1 = lds_ld4(xs_r + 64);
      v4f a0 = mfma(wqf[0], x0, z4), a1 = mfma(wqf[2], x0, z4);
      a0 = mfma(wqf[1], x1, a0); a1 = mfma(wqf[3], x1, a1);
      if (ln < 2) ws_st4(rs, q_st, lay, tag4(ln ? a1 : a0, tag));
      if (qsub == 0 && w == 0) {
        v4f sxx = mfma(x0, x0, z4); sxx = mfma(x1, x1, sxx);
        v4f sx = mfma(ones, x0, z4); sx = mfma(ones, x1, sx);
        const float h0 = u2f(f2u(sx.x) & 0xffffff00u), h1 = u2f(f2u(sxx.x) & 0xffffff00u);
        v4f st = {h0, sx.x - h0, h1, sxx.x - h1};
        if (L == 0) st = v4f{0.f, 0.f, 64.f, 0.f};
        if (l == 0) ws_st4(rs, 0, lay + qstat_off, tag4(st, tag));
      }
      if (L == 0) {
        v4f c0 = mfma(wqf[0], ones, z4), c1 = mfma(wqf[2], ones, z4);
        c0 = mfma(wqf[1], ones, c0); c1 = mfma(wqf[3], ones, c1);
        if (ln < 2) ws_st4(rs, q_st, WS_CDELTA, tag4(ln ? c1 : c0, tag));
      }
    }
    STAMP(2 + 10 * L);
    float rden = 1.f;
    if (L == 0) {
      if (lo) {
        unsigned ko = koff; PIN(ko);
#define LDK2(i) asm volatile("global_load_dwordx4 %0, %1, %2 offset:%3 nt" : "+v"(kf[i]) : "v"(ko), "s"(KB((i) >> 2)), "i"(((i) & 3) * 128) : "memory")
        LDK2(6); LDK2(7); LDK2(8); LDK2(9); LDK2(10); LDK2(11); LDK2(12); LDK2(13); LDK2(14); LDK2(15);
      }
    } else {
      if (isQ) { if (lo) nap<D_Q>(); else nap<D_QH>(); }
    }
    for (int rep = 0; rep < (L == 0 ? (lo ? 2 : 0) : 1); ++rep) {
      v4f qx[2];
      const unsigned d1 = rep * 1024u;
      poll_2x4(rs, qo0 + d1, lg < 3 ? qo1 + d1 : qo1, lay, tag, qx);
      v4f a4 = qx[0]; if (lg < 3) a4 += qx[1];
      const float U = tred4(a4);
      float sx = (ln < 7) ? qx[1].x + qx[1].y : 0.f, sxx = (ln < 7) ? qx[1].z + qx[1].w : 0.f;
      sx = row_sum(sx); sxx = row_sum(sxx);
      float m0; ln_scalars(rdlane(sx, 48), rdlane(sxx, 48), m0, rden);
      const float u = U - m0 * cq;
      *(LAS u16*)(q_lw + rep * 640) = (u16)pk_bf16(u, u);
    }
    if (L == 0 && lo)
      asm volatile("s_waitcnt vmcnt(0)" : "+v"(kf[0]), "+v"(kf[1]), "+v"(kf[2]), "+v"(kf[3]), "+v"(kf[4]), "+v"(kf[5]), "+v"(kf[6]), "+v"(kf[7]),
                   "+v"(kf[8]), "+v"(kf[9]), "+v"(kf[10]), "+v"(kf[11]), "+v"(kf[12]), "+v"(kf[13]), "+v"(kf[14]), "+v"(kf[15]) :: "memory");
    STAMP(3 + 10 * L);
    BAR();
    if (lo) {
      v4f d[4] = {z4, z4, z4, z4};
#pragma unroll
      for (int s = 0; s < 4; ++s) {
        const v4u qf = lds_ld4(qa + s * 128);
#pragma unroll
        for (int T = 0; T < 4; ++T) d[T] = mfma(kf[4 * T + s], qf, d[T]);
      }
      float t[8];
      const bool rb = L == 0 ? RB0 : RB1;
#pragma unroll
      for (int r = 0; r < 8; ++r) {
        auto sw = __builtin_amdgcn_permlane32_swap(f2u(d[(r >> 2) * 2][r & 3]), f2u(d[(r >> 2) * 2 + 1][r & 3]), false, false);
        float s = (u2f(sw[0]) + dpp<QP_X2>(u2f(sw[1]))) * rden;
        if (RB0 != RB1) s = rb ? rbf(s) : s; else if (RB0) s = rbf(s);
        t[r] = s * (1.4426950408889634f / 16.0f);
      }
      const float m = rows_max(fmx(fmx(fmx(t[0], t[1]), fmx(t[2], t[3])), fmx(fmx(t[4], t[5]), fmx(t[6], t[7]))));
      float pr[8];
#pragma unroll
      for (int r = 0; r < 8; ++r) pr[r] = __builtin_amdgcn_exp2f(t[r] - m);
      const v4u pp = {pk_bf16(pr[0], pr[1]), pk_bf16(pr[2], pr[3]), pk_bf16(pr[4], pr[5]), pk_bf16(pr[6], pr[7])};
      if (ln < 2) *(LAS v4u*)p_w = pp;
      const float ls = rows_sum(((pr[0] + pr[1]) + (pr[2] + pr[3])) + ((pr[4] + pr[5]) + (pr[6] + pr[7])));
      if (l < 2) { v2f st = {m, ls}; *(LAS v2f*)(L_S + w * 16 + l * 8) = st; }
    } else if (L == 0) {
      VMWAIT(0);
    }
    STAMP(4 + 10 * L);
    BAR();
    if (lo) {
      if (L == 0) {
        unsigned a = L_V + (4 * lg + trl) * 512 + trp; PIN(a);
        const unsigned h = (unsigned)(4 * (lg & 1)) | trl;
#pragma unroll
        for (int t = 0; t < 4; ++t) {
          const unsigned at = a + (((unsigned)(4 * w + t) ^ h) * 32);
#pragma unroll
          for (int tau = 0; tau < 4; ++tau) vf[t * 4 + tau] = frag(at + tau * 16384, at + tau * 16384 + 8192);
        }
      }
      STAMP(5 + 10 * L);
      const v2f st = *(LAS v2f*)s_r;
      v4u pf[8];
#pragma unroll
      for (int v = 0; v < 8; ++v) pf[v] = lds_ld4(pa + 16 * v);
      float M = st.x; M = fmx(M, dpp<ROR4>(M)); M = fmx(M, dpp<ROR8>(M));
      M = u2f(f2u(M) & 0xffffff00u);
      const float fs = __builtin_amdgcn_exp2f(st.x - M);
      float ls = fs * st.y; ls += dpp<ROR4>(ls); ls += dpp<ROR8>(ls);
      if (w == 0 && l < 2) { v4u sv = {tagv(M, tag), tagv(ls, tag), ep1, ep1}; ws_st4(rs, (unsigned)l * 16u, lay + WS_OSTAT + (unsigned)(g * 32 + j) * 128u, sv); }
      v4f oa = z4, ob = z4;
#pragma unroll
      for (int tau = 0; tau < 4; ++tau)
#pragma unroll
        for (int t2 = 0; t2 < 2; ++t2) { oa = mfma(vf[t2 * 4 + tau], pf[2 * tau + t2], oa); ob = mfma(vf[(2 + t2) * 4 + tau], pf[2 * tau + t2], ob); }
      v4f o;
      float ta[4], tb[4];
#pragma unroll
      for (int r = 0; r < 4; ++r) { ta[r] = oa[r] * fs; tb[r] = ob[r] * fs; }
      asm volatile("" : "+v"(ta[0]), "+v"(ta[1]), "+v"(ta[2]), "+v"(ta[3]), "+v"(tb[0]), "+v"(tb[1]), "+v"(tb[2]), "+v"(tb[3]));
#pragma unroll
      for (int r = 0; r < 4; ++r) { ta[r] += dpp<ROR8>(ta[r]); tb[r] += dpp<ROR8>(tb[r]); }
#pragma unroll
      for (int r = 0; r < 4; ++r) ta[r] = (ln & 8) ? tb[r] : ta[r];
#pragma unroll
      for (int r = 0; r < 4; ++r) ta[r] += dpp<SHL4>(ta[r]);
      asm volatile("" : "+v"(ta[0]), "+v"(ta[1]), "+v"(ta[2]), "+v"(ta[3]));
      o = v4f{ta[0], ta[1], ta[2], ta[3]};
      if ((ln & 4) == 0) ws_st4(rs, o_st, lay, tag4(o, tag));
    } else if (L == 0) {
      int wv = w & 3; asm volatile("" : "+s"(wv));
      unsigned ll = (unsigned)l; PIN(ll);
      if (isO) {
        const u16* wob = p.wo + (size_t)(256 * (2 * go + qsub) + 64 * wv) * D + 64 * qi;
        const unsigned vo = (ll >> 3) * 896u + (ll & 7) * 16u;
#pragma unroll
        for (int u = 0; u < 8; ++u) dma16(wob + u * 8 * D, vo, L_W2 + (8 * wv + u) * 1024);
      } else if (isF) {
        const unsigned vo = (ll >> 1) * 3584u + (ll & 1) * 16u;
#pragma unroll
        for (int u = 0; u < 4; ++u) {
          const int pc = (4 * u + wv) < 14 ? 4 * u + wv : 13;
          dma16(p.wg + (size_t)(32 * pc) * FF + 16 * f, vo, L_W2 + pc * 1024);
          dma16(p.wu + (size_t)(32 * pc) * FF + 16 * f, vo, L_W2 + 14336 + pc * 1024);
          dma16(p.wd + (size_t)(16 * f) * D + pc * 512, ll * 16u, L_W2 + 28672 + pc * 1024);
        }
      }
      VMWAIT(0);
    }
    STAMP(6 + 10 * L);
    if (L == 0) BAR();
    if (L == 0 && j == 31 && w == 7) asm volatile("buffer_wbl2 sc1\n\ts_waitcnt vmcnt(0)" ::: "memory");
    if (isO) {
      v4u w2f[8];
#pragma unroll
      for (int s = 0; s < 8; ++s) w2f[s] = frag(w2_a + s * 4096, w2_a + s * 4096 + 512);
      v4u os;
      for (unsigned sp = 0; sp < SPIN_CAP; ++sp) {
        asm volatile("" ::: "memory");
        os = ws_ld4(rs, r1, lay);
        if (__all((((os.x ^ tag) | (os.y ^ tag)) & 0xffu) == 0u && os.z == ep1 && os.w == ep1)) break;
      }
      nap<D_O>();
      const float Ms = u2f(os.x ^ tag), lsv = u2f(os.y ^ tag);
      float m = fmx(Ms, dpp<QP_X1>(Ms)); m = fmx(m, dpp<QP_X2>(m));
      const float e = __builtin_amdgcn_exp2f(Ms - m);
      float ls = e * lsv; ls += dpp<QP_X1>(ls); ls += dpp<QP_X2>(ls);
      if (l == 0) { v2f st = {m, ls}; *(LAS v2f*)(L_OS + w * 8) = st; }
      const float e0 = dpp<0x00>(e), e1 = dpp<0x55>(e), e2 = dpp<0xAA>(e), e3 = dpp<0xFF>(e);
      v4f ox[4];
      poll_4x4<2048>(rs, r0, lay, tag, ox);
      STAMP(7 + 10 * L);
      const v4f P = (ox[0] * e0 + ox[1] * e1) + (ox[2] * e2 + ox[3] * e3);
      const v2u pp = {pk_bf16(P.x, P.y), pk_bf16(P.z, P.w)};
      *(LAS v2u*)r2 = pp;
      BAR();
      if (lo) {
        const v2f st = *(LAS v2f*)(L_OS + (ln & 7) * 8);
        v4f acc = z4;
#pragma unroll
        for (int s = 0; s < 8; ++s) acc = mfma(w2f[s], lds_ld4(r3 + s * 64), acc);
        float M = st.x; M = fmx(M, dpp<ROR1>(M)); M = fmx(M, dpp<ROR2>(M)); M = fmx(M, dpp<ROR4>(M));
        const float fs = __builtin_amdgcn_exp2f(st.x - M);
        float Ls = fs * st.y; Ls += dpp<ROR1>(Ls); Ls += dpp<ROR2>(Ls); Ls += dpp<ROR4>(Ls);
        const float sc = fs * __builtin_amdgcn_rcpf(Ls);
        float y[4];
#pragma unroll
        for (int r = 0; r < 4; ++r) y[r] = acc[r] * sc;
        asm volatile("" : "+v"(y[0]), "+v"(y[1]), "+v"(y[2]), "+v"(y[3]));
#pragma unroll
        for (int r = 0; r < 4; ++r) y[r] += dpp<ROR1>(y[r]);
#pragma unroll
        for (int r = 0; r < 4; ++r) y[r] += dpp<ROR2>(y[r]);
#pragma unroll
        for (int r = 0; r < 4; ++r) y[r] += dpp<ROR4>(y[r]);
        asm volatile("" : "+v"(y[0]), "+v"(y[1]), "+v"(y[2]), "+v"(y[3]));
        if (ln == 0) ws_st4(rs, r4, lay, tag4(v4f{y[0], y[1], y[2], y[3]}, tag));
      } else if (w == 4 && qi == 0) {
        if (l == 0) ws_st1(rs, 0, lay + WS_XFLAG + (unsigned)(2 * go + qsub) * 128u, ep1);
      }
      STAMP(8 + 10 * L);
    } else if (isF) {
      v4u w2f[6];
#pragma unroll
      for (int s = 0; s < 2; ++s) { w2f[s] = frag(w2_a + s * 1024, w2_a + s * 1024 + 128); w2f[2 + s] = frag(w2_a + s * 1024 + 14336, w2_a + s * 1024 + 14336 + 128); }
      w2f[4] = frag(wd_a, wd_a + 4 * 896); w2f[5] = frag(wd_a + 8 * 896, wd_a + 12 * 896);
      if (L == 0) {
        v4f cg = mfma(w2f[0], ones, z4); cg = mfma(w2f[1], ones, cg);
        v4f cu = mfma(w2f[2], ones, z4); cu = mfma(w2f[3], ones, cu);
        if (w < 7 && ln < 2) *(LAS v4f*)r2 = ln ? cu : cg;
        BAR();
#pragma unroll
        for (int b = 0; b < 7; ++b) cgu += lds_ldf(r3 + b * 144);
      }
      if (w == 7) {
        for (unsigned sp = 0; sp < SPIN_CAP; ++sp) {
          asm volatile("" ::: "memory");
          if (__all(ws_ld1(rs, (unsigned)ln * 128u, lay + WS_XFLAG) == ep1)) break;
        }
        nap<D_X1>();
      } else if (L == 1) {
        xres = u2f(poll1(rs, r1, WS_X2, tag));
      }
      BAR();
      if (w < 7) {
        v4f xx[4];
        poll_4x4<4 * 1792>(rs, r0, lay, tag, xx);
        STAMP(7 + 10 * L);
        const float att = tred4((xx[0] + xx[1]) + (xx[2] + xx[3]));
        const float x1 = rbf(xres + rbf(att));
        *(LAS u16*)xs_w = (u16)(f2u(x1) >> 16);
        if (f == 0) ws_st1(rs, r1, lay + WS_X1, f2u(x1) | tag);
        LGKM0();
        const v4u x0 = lds_ld4(xs_r), x1f = lds_ld4(xs_r + 64);
        v4f ga = mfma(w2f[0], x0, z4); v4f ua = mfma(w2f[2], x0, z4);
        v4f sxx = mfma(x0, x0, z4); v4f sx = mfma(ones, x0, z4);
        ga = mfma(w2f[1], x1f, ga); ua = mfma(w2f[3], x1f, ua);
        sxx = mfma(x1f, x1f, sxx); sx = mfma(ones, x1f, sx);
        if (ln < 2) *(LAS v4f*)r2 = ln ? ua : ga;
        if (l == 2) { v2f st = {sx.x, sxx.x}; *(LAS v2f*)(L_G + w * 144 + 128) = st; }
      }
      BAR();
      if (w < 7) {
        float sv[7];
#pragma unroll
        for (int b = 0; b < 7; ++b) sv[b] = lds_ldf(r3 + b * 144);
        const float s = ((sv[0] + sv[1]) + (sv[2] + sv[3])) + ((sv[4] + sv[5]) + sv[6]);
        float m, rd; ln_scalars(rdlane(s, 32), rdlane(s, 48), m, rd);
        const float val = rbf((s - m * cgu) * rd);
        auto sw = __builtin_amdgcn_permlane16_swap(f2u(val), f2u(val), false, false);
        const float gg = u2f(sw[0]), uu = u2f(sw[1]);
        const float sg = rbf(gg * __builtin_amdgcn_rcpf(1.0f + __builtin_amdgcn_exp2f(gg * -1.4426950408889634f)));
        const float h = sg * uu;
        const unsigned hp = pk_bf16(h, dpp<QP_X1>(h));
        float d0 = 0.f, d1 = 0.f;
#pragma unroll
        for (int k = 0; k < 4; ++k) {
          d0 = dot2(w2f[4][k], (unsigned)__builtin_amdgcn_readlane((int)hp, 2 * k), d0);
          d1 = dot2(w2f[5][k], (unsigned)__builtin_amdgcn_readlane((int)hp, 8 + 2 * k), d1);
        }
        ws_st1(rs, r4, lay, tagv(d0 + d1, tag));
      }
      STAMP(8 + 10 * L);
    }
    if (L == 0) {
      v4f cx[2];
      poll_2x4(rs, qo0, lg < 3 ? qo1 : qo0, WS_CDELTA, tag, cx);
      v4f c4v = cx[0]; if (lg < 3) c4v += cx[1];
      cq = tred4(c4v);
    }
    if (L == 1 && !(g == 0 && j < 7)) break;
    READ_WQF();
    const float x1 = u2f(poll1(rs, x1o, lay, tag));
    if (isQ) {
      nap<D_D>();
      if (w < 7) {
        v4f dx[4];
        poll_4x4<4 * 1792>(rs, dofs, lay, tag, dx);
        *(LAS float*)(red_a + w * 256) = tred4((dx[0] + dx[1]) + (dx[2] + dx[3]));
      }
      STAMP(9 + 10 * L);
      BAR();
      float sv[7];
#pragma unroll
      for (int b = 0; b < 7; ++b) sv[b] = lds_ldf(red_a + b * 256);
      const float s = ((sv[0] + sv[1]) + (sv[2] + sv[3])) + ((sv[4] + sv[5]) + sv[6]);
      xv = rbf(x1 + rbf(s));
      if (L == 1) {
        if (w == 0) {
          p.out[64 * qi + c4] = (u16)(f2u(xv) >> 16);
          if (j == 0 && l == 0) ws_st1(rs, 0, WS_EPOCH, ep1);
        }
        STAMP(20);
        break;
      }
      if (g == 0 && qsub == 0 && w == 4) ws_st1(rs, x1o, WS_X2 - WS_X1, f2u(xv) | tag);
    } else {
      nap<D_QS>();
    }
  }
}
}

extern "C" void kernel_launch(void* const* d_in, const int* in_sizes, int n_in, void* d_out, int out_size, void* d_ws, size_t ws_size,
                              hipStream_t stream) {
  Params p;
  p.x = (const u16*)d_in[0]; p.kc = (const u16*)d_in[1]; p.vc = (const u16*)d_in[2]; p.wq = (const u16*)d_in[3]; p.wo = (const u16*)d_in[4];
  p.wg = (const u16*)d_in[6]; p.wu = (const u16*)d_in[7]; p.wd = (const u16*)d_in[8];
  p.out = (u16*)d_out; p.ws = (unsigned char*)d_ws;
  (void)hipFuncSetAttribute((const void*)fused_decode, hipFuncAttributeMaxDynamicSharedMemorySize, (int)LDS_BYTES);
  hipLaunchKernelGGL(fused_decode, dim3(256), dim3(512), LDS_BYTES, stream, p);
}
```

```cpp
#include <hip/hip_runtime.h>
#include <stdint.h>
#include <type_traits>
#pragma clang diagnostic ignored "-Wint-to-pointer-cast"

typedef unsigned short u16;
typedef unsigned long long u64;
typedef unsigned v4u __attribute__((ext_vector_type(4)));
typedef unsigned v2u __attribute__((ext_vector_type(2)));
typedef float v4f __attribute__((ext_vector_type(4)));
typedef float v2f __attribute__((ext_vector_type(2)));
typedef short v4s __attribute__((ext_vector_type(4)));
typedef __bf16 v8bf __attribute__((ext_vector_type(8)));
typedef __bf16 v2bf __attribute__((ext_vector_type(2)));
#define LAS __attribute__((address_space(3)))
#define GAS __attribute__((address_space(1)))
#define DEVI __device__ __forceinline__
#define UNLIKELY(x) __builtin_expect(!!(x), 0)

#define STAMPS 0
#define SHIFT_Q 1
#define SHIFT_O 1
#define TLB_GRAN 0
#define NOHINT_O 1
#define NOHINT_Q 1
#define FW_EARLY 0
#define EXTRA_LATE 1
#define GU_NT false
#define GU_WARM 0
#define PRETOUCH 0
#define XH_EARLY 1
#define QH_EARLY 1
#define TUNE 0
enum { T_OH0, T_OH1, T_O, T_X1, T_D0, T_D1, T_Q, T_FW, T_K, T_XH, T_QH, T_DF, T_N = 16 };
#define DEFAULT_TUNE {6, 6, 0, 4, 11, 10, 0, 10, 12, 9, 24, 15, 0, 0, 0, 0}

namespace {
constexpr int D = 448, SK = 65536, WIN = 4096, FF = 1792;
constexpr unsigned WS_EPOCH = 0;
constexpr unsigned WS_QREC = 4096;
constexpr unsigned WS_QSTAT = WS_QREC + 8 * 7 * 2048;
constexpr unsigned WS_QHINT = WS_QSTAT + 8 * 7 * 128;
constexpr unsigned WS_OREC = WS_QHINT + 8 * 28 * 128;
constexpr unsigned WS_OSTAT = WS_OREC + 256 * 2048;
constexpr unsigned WS_OHINT = WS_OSTAT + 256 * 128;
constexpr unsigned WS_XREC = WS_OHINT + 256 * 128;
constexpr unsigned WS_X1 = WS_XREC + 16 * 1792;
constexpr unsigned WS_X2 = WS_X1 + 1792;
constexpr unsigned WS_XHINT = WS_X2 + 1792;
constexpr unsigned WS_DREC = WS_XHINT + 16 * 128;
constexpr unsigned WS_L = 1u << 20;
static_assert(WS_DREC + 112 * 1792 <= WS_L, "layer block");
constexpr unsigned WS_CDELTA = 2 * WS_L;
constexpr unsigned WS_STAMP = 4u << 20;
constexpr unsigned L_V = 0;
constexpr unsigned L_WQ = 65536;
constexpr unsigned L_W2 = 81920;
constexpr unsigned QSTR = 1344;
constexpr unsigned L_QZ = L_W2 + 43008;
constexpr unsigned L_Q = L_QZ + 3 * QSTR;
constexpr unsigned L_ZERO = L_QZ;
constexpr unsigned L_P = L_QZ + 7 * QSTR;
constexpr unsigned L_S = L_P + 512;
constexpr unsigned L_OP = L_S + 64;
constexpr unsigned L_OS = L_OP + 8 * 528;
constexpr unsigned L_XS = L_OS + 64;
constexpr unsigned L_RED = L_XS + 1024;
constexpr unsigned L_G = L_RED + 1792;
constexpr unsigned L_END = L_G + 14 * 144;
constexpr unsigned L_STAMP = (L_END + 15) / 16 * 16;
constexpr unsigned LDS_BYTES = L_STAMP + 512;
static_assert(LDS_BYTES <= 160 * 1024 && L_QZ % 16 == 0 && L_P % 16 == 0 && L_OP % 16 == 0 && L_XS % 16 == 0 && L_G % 16 == 0, "lds");
constexpr unsigned SPIN_CAP = 1u << 16;
#define CODE_PF (STAMPS == 1 ? 27136 : 22272)
constexpr unsigned CODE_PF_BYTES = CODE_PF;

struct Params { const u16 *x, *kc, *vc, *wq, *wo, *wg, *wu, *wd; u16* out; unsigned char* ws;
};
constexpr int kTune[T_N] = DEFAULT_TUNE;
#define NAP(i) nap<kTune[i]>()
typedef __amdgpu_buffer_rsrc_t rsrc_t;

DEVI float u2f(unsigned u) { return __builtin_bit_cast(float, u); }
DEVI unsigned f2u(float f) { return __builtin_bit_cast(unsigned, f); }
DEVI unsigned pk_bf16(float lo, float hi) { v2f f = {lo, hi}; return __builtin_bit_cast(unsigned, __builtin_convertvector(f, v2bf)); }
DEVI float rbf(float f) { return u2f(pk_bf16(0.f, f)); }
DEVI v4f mfma(v4u a, v4u b, v4f c) { return __builtin_amdgcn_mfma_f32_16x16x32_bf16(__builtin_bit_cast(v8bf, a), __builtin_bit_cast(v8bf, b), c, 0, 0, 0); }
DEVI float dot2(unsigned a, unsigned b, float c) { return __builtin_amdgcn_fdot2_f32_bf16(__builtin_bit_cast(v2bf, a), __builtin_bit_cast(v2bf, b), c, false); }
template <int CTRL> DEVI float dpp(float x) { return u2f((unsigned)__builtin_amdgcn_mov_dpp((int)f2u(x), CTRL, 0xf, 0xf, true)); }
constexpr int ROR1 = 0x121, ROR2 = 0x122, ROR4 = 0x124, ROR8 = 0x128, SHL4 = 0x104, QP_X1 = 0xB1, QP_X2 = 0x4E;
#define DPPMAX(x, ctrl) asm("s_nop 1\n\tv_max_f32_dpp %0, %0, %0 " ctrl " row_mask:0xf bank_mask:0xf" : "+v"(x))
DEVI float red32(float a, float b) { auto r = __builtin_amdgcn_permlane32_swap(f2u(a), f2u(b), false, false); return u2f(r[0]) + u2f(r[1]); }
DEVI float red16(float a, float b) { auto r = __builtin_amdgcn_permlane16_swap(f2u(a), f2u(b), false, false); return u2f(r[0]) + u2f(r[1]); }
DEVI float tred4(v4f a) { return red16(red32(a.x, a.z), red32(a.y, a.w)); }
DEVI float rows_max(float x) {
  float t;
  asm("v_mov_b32 %1, %0\n\ts_nop 1\n\tv_permlane32_swap_b32 %0, %1\n\tv_max_f32 %0, %0, %1\n\t"
      "v_mov_b32 %1, %0\n\ts_nop 1\n\tv_permlane16_swap_b32 %0, %1\n\tv_max_f32 %0, %0, %1" : "+v"(x), "=&v"(t));
  return x;
}
DEVI float rows_sum(float x) { x = red32(x, x); return red16(x, x); }
DEVI float row_sum(float x) { x += dpp<ROR8>(x); x += dpp<ROR4>(x); x += dpp<ROR2>(x); x += dpp<ROR1>(x); return x; }
DEVI float rdlane(float x, int lane) { return u2f((unsigned)__builtin_amdgcn_readlane((int)f2u(x), lane)); }
DEVI unsigned tagv(float v, unsigned tag) { return __builtin_amdgcn_perm(f2u(v), tag, 0x07060500u); }
DEVI v4u tag4(v4f v, unsigned tag) { v4u r = {tagv(v.x, tag), tagv(v.y, tag), tagv(v.z, tag), tagv(v.w, tag)}; return r; }
DEVI v4f untag4(v4u x, unsigned tag, unsigned& bad) { x ^= tag; bad |= (x.x | x.y) | (x.z | x.w); return __builtin_bit_cast(v4f, x); }
#define BAR() asm volatile("s_waitcnt lgkmcnt(0)\n\ts_barrier" ::: "memory")
#define LGKM0() asm volatile("s_waitcnt lgkmcnt(0)" ::: "memory")
#define VMWAIT(n) asm volatile("s_waitcnt vmcnt(%0)" :: "i"(n) : "memory")
#define PIN(x) asm volatile("" : "+v"(x))
#define SCHED() __builtin_amdgcn_sched_barrier(0)
template <int N> DEVI void nap() { if (N > 0) __builtin_amdgcn_s_sleep(N); }

template <bool NT = true> DEVI void dma16(const void* sbase, unsigned voff, unsigned ldsaddr) {
  unsigned keep;
  if (NT) asm volatile("s_mov_b32 %0, m0\n\ts_mov_b32 m0, %3\n\ts_nop 3\n\tglobal_load_lds_dwordx4 %1, %2 nt\n\ts_mov_b32 m0, %0"
               : "=&s"(keep) : "v"(voff), "s"(sbase), "s"(ldsaddr) : "memory");
  else asm volatile("s_mov_b32 %0, m0\n\ts_mov_b32 m0, %3\n\ts_nop 3\n\tglobal_load_lds_dwordx4 %1, %2\n\ts_mov_b32 m0, %0"
               : "=&s"(keep) : "v"(voff), "s"(sbase), "s"(ldsaddr) : "memory");
}
DEVI void dma16x4(const void* sbase, unsigned va, unsigned vb, unsigned ldsaddr) {
  unsigned keep;
  asm volatile("s_mov_b32 %0, m0\n\ts_mov_b32 m0, %4\n\ts_nop 3\n\t"
               "global_load_lds_dwordx4 %1, %3 nt\n\tglobal_load_lds_dwordx4 %2, %3 offset:1024 nt\n\t"
               "global_load_lds_dwordx4 %1, %3 offset:2048 nt\n\tglobal_load_lds_dwordx4 %2, %3 offset:3072 nt\n\ts_mov_b32 m0, %0"
               : "=&s"(keep) : "v"(va), "v"(vb), "s"(sbase), "s"(ldsaddr) : "memory");
}
template <bool NT = false> DEVI void touch(const void* sbase, unsigned voff, unsigned ldsaddr, bool real = true) {
  unsigned keep;
  if (!real) voff = 0;
  if (NT) asm volatile("s_mov_b32 %0, m0\n\ts_mov_b32 m0, %3\n\ts_nop 3\n\tglobal_load_lds_dword %1, %2 nt\n\ts_mov_b32 m0, %0"
               : "=&s"(keep) : "v"(voff), "s"(sbase), "s"(ldsaddr) : "memory");
  else asm volatile("s_mov_b32 %0, m0\n\ts_mov_b32 m0, %3\n\ts_nop 3\n\tglobal_load_lds_dword %1, %2\n\ts_mov_b32 m0, %0"
               : "=&s"(keep) : "v"(voff), "s"(sbase), "s"(ldsaddr) : "memory");
}
DEVI v4u ws_ld4(rsrc_t rs, unsigned vo, unsigned so) { return __builtin_amdgcn_raw_buffer_load_b128(rs, vo, so, 16); }
DEVI unsigned ws_ld1(rsrc_t rs, unsigned vo, unsigned so) { return __builtin_amdgcn_raw_buffer_load_b32(rs, vo, so, 16); }
DEVI void ws_st4(rsrc_t rs, unsigned vo, unsigned so, v4u v) { __builtin_amdgcn_raw_buffer_store_b128(v, rs, vo, so, 16); }
DEVI void ws_st1(rsrc_t rs, unsigned vo, unsigned so, unsigned v) { __builtin_amdgcn_raw_buffer_store_b32(v, rs, vo, so, 16); }
DEVI v4u lds_ld4(unsigned a) { return *(LAS v4u*)a; }
DEVI float lds_ldf(unsigned a) { return *(LAS float*)a; }
DEVI v2u tr_read(unsigned a) { return __builtin_bit_cast(v2u, __builtin_amdgcn_ds_read_tr16_b64_v4i16((LAS v4s*)a)); }
DEVI v4u frag(unsigned a0, unsigned a1) { v2u lo = tr_read(a0), hi = tr_read(a1); v4u r = {lo.x, lo.y, hi.x, hi.y}; return r; }
DEVI bool anybad(unsigned bad) { return __builtin_amdgcn_ballot_w64((bad & 0xffu) != 0u) != 0ull; }

#define LIKELY(x) __builtin_expect(!!(x), 1)
template <int N, unsigned STR> DEVI void read_units(rsrc_t rs, unsigned vo, unsigned so, unsigned tag, v4f (&y)[N], unsigned& retries) {
#pragma nounroll
  for (unsigned sp = 0;; ++sp) {
    asm volatile("" ::: "memory");
    v4u x[N]; unsigned bad = 0;
#pragma unroll
    for (int k = 0; k < N; ++k) x[k] = ws_ld4(rs, vo, so + k * STR);
#pragma unroll
    for (int k = 0; k < N; ++k) y[k] = untag4(x[k], tag, bad);
    if (LIKELY(!anybad(bad))) break;
    asm volatile("" ::: "memory");
    if (STAMPS == 1) ++retries;
    if (sp >= SPIN_CAP) break;
  }
}
template <int NAP> DEVI void read_2(rsrc_t rs, unsigned v0, unsigned v1, unsigned so, unsigned tag, v4f (&y)[2], unsigned& retries) {
#pragma nounroll
  for (unsigned sp = 0;; ++sp) {
    asm volatile("" ::: "memory");
    unsigned bad = 0;
    const v4u x0 = ws_ld4(rs, v0, so), x1 = ws_ld4(rs, v1, so);
    y[0] = untag4(x0, tag, bad); y[1] = untag4(x1, tag, bad);
    if (LIKELY(!anybad(bad))) break;
    asm volatile("" ::: "memory");
    if (STAMPS == 1) ++retries;
    nap<NAP>();
    if (sp >= SPIN_CAP) break;
  }
}
DEVI unsigned poll1(rsrc_t rs, unsigned vo, unsigned so, unsigned tag) {
  unsigned x;
#pragma nounroll
  for (unsigned sp = 0;; ++sp) {
    asm volatile("" ::: "memory");
    x = ws_ld1(rs, vo, so) ^ tag;
    if (!anybad(x)) break;
    asm volatile("" ::: "memory");
    if (sp >= SPIN_CAP) break;
  }
  return x;
}
DEVI void wait_hint(rsrc_t rs, unsigned vo, unsigned so, unsigned ep1) {
#pragma nounroll
  for (unsigned sp = 0;; ++sp) {
    asm volatile("" ::: "memory");
    if (__builtin_amdgcn_ballot_w64(ws_ld1(rs, vo, so) != ep1) == 0ull) break;
    asm volatile("" ::: "memory");
    if (sp >= SPIN_CAP) break;
  }
}
DEVI float ln_mean(float sx) { return sx * (1.0f / D); }
DEVI float ln_rden(float mean, float sxx) {
  const float var = __builtin_fmaxf(sxx * (1.0f / D) - mean * mean, 0.f);
  return __builtin_amdgcn_rcpf(rbf(__builtin_amdgcn_sqrtf(rbf(rbf(var) + 1e-5f))));
}

#define STAMP(i) do {} while (0)

template <bool LO> DEVI void run(const Params& p, const int w, v4u (&kf)[16], unsigned epoch_, unsigned xr_, unsigned xq_, v4u xall_) {
  const int tid = threadIdx.x, l = tid & 63, lg = l >> 4, ln = l & 15, wv = w & 3;
  const int wg = blockIdx.x, g = wg & 7, j = wg >> 3;
  const int gq = (g + SHIFT_Q) & 7, go = (g + SHIFT_O) & 7;
  const bool isQ = j < 28, isO = j < 14, isF = j >= 14 && j < 28;
  const int qi = j % 7, qsub = j / 7;
  const int f = 14 * g + (j - 14);
  const rsrc_t rs = __builtin_amdgcn_make_buffer_rsrc(p.ws, 0, (int)(8u << 20), 0x00020000);
  const unsigned c4 = (unsigned)(4 * ln + lg);
  const size_t kvrow = (size_t)g * SK + (SK - WIN) + 128 * j;
  const int rot = wv + j + 5 * g;
  const unsigned trl = (unsigned)(ln >> 2), trp = (unsigned)(ln & 3) * 8u;
  const v4u ones = {0x3f803f80u, 0x3f803f80u, 0x3f803f80u, 0x3f803f80u};
  const v4f z4 = {0.f, 0.f, 0.f, 0.f};
  const int wc = w < 7 ? w : 6;
  const unsigned orec = (unsigned)(g * 32 + j);

  unsigned xs_w = L_XS + w * 128 + c4 * 2, xs_r = L_XS + w * 128 + lg * 16;
  unsigned qo0 = WS_QREC + (unsigned)(g * 14336 + lg * 2048 + (64 * w + 4 * ln) * 4);
  unsigned qo1 = lg < 3 ? qo0 + 8192u : WS_QSTAT + (unsigned)(g * 7 + (ln < 7 ? ln : 6)) * 128u;
  unsigned q_lw = L_Q + (w >> 2) * 640 + (wv * 64 + c4) * 2;
  unsigned x1o = WS_X1 + (64u * qi + c4) * 4u;
  unsigned dofs = WS_DREC + (unsigned)((16 * wc + lg) * 1792 + (64 * qi + 4 * ln) * 4);
  unsigned red_a = L_RED + l * 4;
  unsigned a0, a1, a2, a3, a4, a5;
  if (LO) {
    a0 = L_Q + (ln & 1) * 640 + lg * 32 + ((ln >> 1) & 1) * 16 - (ln >> 2) * QSTR;
    a1 = L_P + (((ln >> 2) * 4 + w) * 2 + (ln & 1)) * 16 + (4 * (lg & 1) + (ln & 2) + (lg >> 1)) * 2;
    const unsigned pr = L_P + (lg * 2 + (ln & 1)) * 16;
    a2 = ((ln >> 2) == lg && (ln & 2) == 0) ? pr : L_ZERO;
    a3 = ((ln >> 2) == lg && (ln & 2) != 0) ? pr : L_ZERO;
    a4 = L_S + (ln >> 2) * 8 + (ln & 1) * 4;
    a5 = WS_OREC + (unsigned)((g * 32 + j) * 2048 + (ln & 1) * 1024 + (64 * w + 32 * (ln >> 3) + 16 * ((ln >> 1) & 1) + 4 * lg) * 4);
  } else {
    a0 = WS_QREC + (unsigned)(gq * 14336 + qi * 2048 + (128 * qsub + 32 * wv + 16 * ln + 4 * lg) * 4);
    a1 = L_P + (l & 31) * 16;
    a2 = L_S + ((l >> 1) & 3) * 8 + (l & 1) * 4;
    a3 = a4 = a5 = 0;
  }
  unsigned r0, r1, r2, r3, r4, r5;
  if (isO) {
    r0 = WS_OREC + (unsigned)((go * 32 + 4 * w) * 2048 + qsub * 1024 + l * 16);
    r1 = WS_OSTAT + (unsigned)((go * 32 + 4 * w + (l & 3)) * 128 + qsub * 16);
    r2 = L_OP + w * 528 + l * 8;
    r3 = L_OP + (ln & 7) * 528 + lg * 16;
    r4 = WS_XREC + (unsigned)((2 * go + qsub) * 1792 + (64 * qi + 16 * wv + 4 * lg) * 4);
    r5 = L_OS + (ln & 7) * 8;
  } else {
    r0 = WS_XREC + (unsigned)(lg * 1792 + (64 * wc + 4 * ln) * 4);
    r1 = (64u * wc + c4) * 4u;
    r2 = L_G + wc * 144 + ln * 64 + lg * 16;
    r3 = L_G + (l & 31) * 4;
    r4 = WS_DREC + (unsigned)(f * 1792 + (64 * wc + l) * 4);
    r5 = (unsigned)ln * 128u;
  }
  PIN(xs_w); PIN(xs_r); PIN(qo0); PIN(qo1); PIN(q_lw); PIN(x1o); PIN(dofs); PIN(red_a);
  PIN(a0); PIN(a1); PIN(a2); if (LO) { PIN(a3); PIN(a4); PIN(a5); }
  PIN(r0); PIN(r1); PIN(r2); PIN(r3); PIN(r4); PIN(r5);

  if (LO) {
    if (tid < 3 * QSTR / 16) { *(LAS v4u*)(L_QZ + tid * 16) = v4u{0u, 0u, 0u, 0u}; *(LAS v4u*)(L_QZ + 4 * QSTR + tid * 16) = v4u{0u, 0u, 0u, 0u}; }
  }
  unsigned epoch, xr_raw, xq_raw = 0; v2u xa0 = {0u, 0u}, xa1 = {0u, 0u};
  if (LO) asm volatile("s_waitcnt vmcnt(16)\n\tv_mov_b32 %0, %2\n\tv_mov_b32 %1, %3" : "=&v"(epoch), "=&v"(xr_raw) : "v"(epoch_), "v"(xr_), "v"(xq_), "v"(xall_) : "memory");
  else asm volatile("s_waitcnt vmcnt(4)\n\tv_mov_b32 %0, %5\n\tv_mov_b32 %1, %6\n\tv_mov_b32 %2, %7\n\tv_mov_b64 %3, %8\n\tv_mov_b64 %4, %9"
                    : "=&v"(epoch), "=&v"(xr_raw), "=&v"(xq_raw), "=&v"(xa0), "=&v"(xa1) : "v"(epoch_), "v"(xr_), "v"(xq_), "v"(xall_.lo), "v"(xall_.hi) : "memory");
  const v4u xall = {xa0.x, xa0.y, xa1.x, xa1.y};
  const unsigned ep1 = (unsigned)__builtin_amdgcn_readfirstlane(epoch) + 1u, tag = ep1 & 0xffu;
  const unsigned qstat_off = WS_QSTAT + (unsigned)(gq * 7 + qi) * 128u;
  STAMP(1);
  float xres = u2f(xr_raw << 16);
  float cq = 0.f, cgu = 0.f;
  float rden = 1.f;
  unsigned rt[6] = {0, 0, 0, 0, 0, 0};
  v4u wqf[4], vf[16], w2f[8];

  auto q_mfma = [&](float xv, unsigned lay, v4u& x0, v4u& x1) {
    *(LAS u16*)xs_w = (u16)(f2u(xv) >> 16);
    LGKM0();
    x0 = lds_ld4(xs_r); x1 = lds_ld4(xs_r + 64);
    v4f q0 = mfma(wqf[0], x0, z4), q1 = mfma(wqf[2], x0, z4);
    q0 = mfma(wqf[1], x1, q0); q1 = mfma(wqf[3], x1, q1);
    if (ln < 2) ws_st4(rs, a0, lay, tag4(ln ? q1 : q0, tag));
  };
  auto qk_softmax = [&](bool round_scores) {
    v4f d = z4;
    v4u qf[12];
#pragma unroll
    for (int u = 0; u < 12; ++u) qf[u] = lds_ld4(a0 + (u >> 2) * QSTR + (u & 3) * 128);
    SCHED();
#pragma unroll
    for (int u = 0; u < 4; ++u) { d = mfma(kf[u], qf[u], d); qf[u] = lds_ld4(a0 + 3 * QSTR + u * 128); }
    SCHED();
#pragma unroll
    for (int u = 4; u < 16; ++u) d = mfma(kf[u], qf[u < 12 ? u : u - 12], d);
    auto s01 = __builtin_amdgcn_permlane32_swap(f2u(d.x), f2u(d.y), false, false);
    auto s23 = __builtin_amdgcn_permlane32_swap(f2u(d.z), f2u(d.w), false, false);
    const float sa = u2f(s01[0]) + dpp<QP_X2>(u2f(s01[1])), sb = dpp<QP_X2>(u2f(s23[0])) + u2f(s23[1]);
    float s = (ln & 2) ? sb : sa;
    if (round_scores) s = rbf(s) * (1.4426950408889634f / 16.0f); else s *= rden;
    float m = s;
    DPPMAX(m, "quad_perm:[2,3,0,1]"); DPPMAX(m, "row_ror:4"); DPPMAX(m, "row_ror:8");
    m = rows_max(m);
    const float pr = __builtin_amdgcn_exp2f(s - m);
    *(LAS u16*)a1 = (u16)pk_bf16(pr, pr);
    if (l < 2) *(LAS float*)(L_S + w * 8 + l * 4) = m;
  };
  auto pv = [&](unsigned lay) {
    const float mw = lds_ldf(a4);
    v4u pe[4], po[4];
#pragma unroll
    for (int tau = 0; tau < 4; ++tau) { pe[tau] = lds_ld4(a2 + tau * 128); po[tau] = lds_ld4(a3 + tau * 128); }
    SCHED();
    float M = mw; DPPMAX(M, "row_ror:4"); DPPMAX(M, "row_ror:8");
    M = u2f(f2u(M) & 0xffffff00u);
    const float fs = __builtin_amdgcn_exp2f(mw - M);
    v4f oa = z4, ob = z4;
#pragma unroll
    for (int tau = 0; tau < 4; ++tau) {
      oa = mfma(vf[tau], pe[tau], oa); ob = mfma(vf[8 + tau], pe[tau], ob);
      oa = mfma(vf[4 + tau], po[tau], oa); ob = mfma(vf[12 + tau], po[tau], ob);
    }
    float ta[4], tb[4];
#pragma unroll
    for (int r = 0; r < 4; ++r) { ta[r] = oa[r] * fs; tb[r] = ob[r] * fs; }
    asm volatile("" : "+v"(ta[0]), "+v"(ta[1]), "+v"(ta[2]), "+v"(ta[3]), "+v"(tb[0]), "+v"(tb[1]), "+v"(tb[2]), "+v"(tb[3]));
#pragma unroll
    for (int r = 0; r < 4; ++r) { ta[r] += dpp<ROR8>(ta[r]); tb[r] += dpp<ROR8>(tb[r]); }
#pragma unroll
    for (int r = 0; r < 4; ++r) ta[r] = (ln & 8) ? tb[r] : ta[r];
#pragma unroll
    for (int r = 0; r < 4; ++r) ta[r] += dpp<SHL4>(ta[r]);
    asm volatile("" : "+v"(ta[0]), "+v"(ta[1]), "+v"(ta[2]), "+v"(ta[3]));
    if ((ln & 4) == 0) ws_st4(rs, a5, lay, tag4(v4f{ta[0], ta[1], ta[2], ta[3]}, tag));
  };
  auto ostat = [&](unsigned lay) {
    const v4u pu = lds_ld4(a1);
    const float mw = lds_ldf(a2);
    float M = mw; DPPMAX(M, "quad_perm:[2,3,0,1]"); DPPMAX(M, "row_ror:4");
    M = u2f(f2u(M) & 0xffffff00u);
    float s = dot2(pu.x, 0x3f803f80u, 0.f); s = dot2(pu.y, 0x3f803f80u, s); s = dot2(pu.z, 0x3f803f80u, s); s = dot2(pu.w, 0x3f803f80u, s);
    s *= __builtin_amdgcn_exp2f(mw - M);
    s += dpp<QP_X2>(s); s += dpp<ROR4>(s); s += dpp<ROR8>(s);
    s = red16(s, s);
    if (l < 2) { v4u sv = {tagv(M, tag), tagv(s, tag), ep1, ep1}; ws_st4(rs, (unsigned)l * 16u, lay + WS_OSTAT + orec * 128u, sv); }
  };
  auto o_stage = [&](auto Lc) {
    constexpr int L = decltype(Lc)::value; constexpr unsigned lay = L * WS_L;
    if ((L == 0 || !NOHINT_O) && !LO && w == 6) { wait_hint(rs, (unsigned)(l & 31) * 128u, lay + WS_OHINT + (unsigned)go * 4096u, ep1); NAP(T_O); }
    if (L == 0 && !LO) VMWAIT(0);
    BAR();
    if (L == 1 && NOHINT_O) NAP(T_OH1);
    if (L == 1) STAMP(15); else STAMP(5);
    v4u os; v4f ox[4];
#pragma nounroll
    for (unsigned sp = 0;; ++sp) {
      asm volatile("" ::: "memory");
      v4u x[4]; unsigned bad;
      os = ws_ld4(rs, r1, lay);
#pragma unroll
      for (int k = 0; k < 4; ++k) x[k] = ws_ld4(rs, r0, lay + k * 2048);
      if (L == 0 && !LO) {
        const unsigned a = L_W2 + trp + (8 * lg + trl) * 128 + wv * 32;
#pragma unroll
        for (int s = 0; s < 8; ++s) w2f[s] = frag(a + s * 4096, a + s * 4096 + 512);
      }
      os.x ^= tag; os.y ^= tag; bad = os.x | os.y | ((os.z ^ ep1) ? 1u : 0u);
#pragma unroll
      for (int k = 0; k < 4; ++k) ox[k] = untag4(x[k], tag, bad);
      if (LIKELY(!anybad(bad))) break;
      asm volatile("" ::: "memory");
      if (STAMPS == 1) ++rt[L];
      if (sp >= SPIN_CAP) break;
    }
    if (XH_EARLY && LO && w == 3 && qi == 0 && l == 0) ws_st1(rs, 0, lay + WS_XHINT + (unsigned)(2 * go + qsub) * 128u, ep1);
    STAMP(7 + 10 * L);
    const float Ms = u2f(os.x);
    float m = Ms; DPPMAX(m, "quad_perm:[1,0,3,2]"); DPPMAX(m, "quad_perm:[2,3,0,1]");
    const float e = __builtin_amdgcn_exp2f(Ms - m);
    const v4f P = (ox[0] * dpp<0x00>(e) + ox[1] * dpp<0x55>(e)) + (ox[2] * dpp<0xAA>(e) + ox[3] * dpp<0xFF>(e));
    const v2u pp = {pk_bf16(P.x, P.y), pk_bf16(P.z, P.w)};
    *(LAS v2u*)r2 = pp;
    float ls = e * u2f(os.y); ls += dpp<QP_X1>(ls); ls += dpp<QP_X2>(ls);
    if (l == 0) { v2f st = {m, ls}; *(LAS v2f*)(L_OS + w * 8) = st; }
    BAR();
    if (!LO) {
      const v2f st = *(LAS v2f*)r5;
      v4u bf[8];
#pragma unroll
      for (int s = 0; s < 8; ++s) bf[s] = lds_ld4(r3 + s * 64);
      SCHED();
      float M = st.x; DPPMAX(M, "row_ror:1"); DPPMAX(M, "row_ror:2"); DPPMAX(M, "row_ror:4");
      const float fs = __builtin_amdgcn_exp2f(st.x - M);
      float Ls = fs * st.y; PIN(Ls); Ls += dpp<ROR1>(Ls); Ls += dpp<ROR2>(Ls); Ls += dpp<ROR4>(Ls);
      const float sc = fs * __builtin_amdgcn_rcpf(Ls);
      v4f acc = z4;
#pragma unroll
      for (int s = 0; s < 8; ++s) acc = mfma(w2f[s], bf[s], acc);
      float y[4];
#pragma unroll
      for (int r = 0; r < 4; ++r) y[r] = acc[r] * sc;
      asm volatile("" : "+v"(y[0]), "+v"(y[1]), "+v"(y[2]), "+v"(y[3]));
#pragma unroll
      for (int r = 0; r < 4; ++r) y[r] += dpp<ROR1>(y[r]);
#pragma unroll
      for (int r = 0; r < 4; ++r) y[r] += dpp<ROR2>(y[r]);
#pragma unroll
      for (int r = 0; r < 4; ++r) y[r] += dpp<ROR4>(y[r]);
      asm volatile("" : "+v"(y[0]), "+v"(y[1]), "+v"(y[2]), "+v"(y[3]));
      if (ln == 0) ws_st4(rs, r4, lay, tag4(v4f{y[0], y[1], y[2], y[3]}, tag));
    } else if (!XH_EARLY && w == 0 && qi == 0) {
      if (l == 0) ws_st1(rs, 0, lay + WS_XHINT + (unsigned)(2 * go + qsub) * 128u, ep1);
    }
    STAMP(8 + 10 * L);
  };
  auto f_stage = [&](auto Lc) {
    constexpr int L = decltype(Lc)::value; constexpr unsigned lay = L * WS_L;
    if (L == 1) xres = u2f(poll1(rs, r1, WS_X2, tag));
    if (L == 0 ? (LO && w == 0) : (!LO && w == 7)) { wait_hint(rs, r5, lay + WS_XHINT, ep1); NAP(T_X1); }
    if (L == 0 && !LO) VMWAIT(0);
    BAR();
    if (L == 1) STAMP(15); else STAMP(5);
    if (LO || w < 7) {
      v4f xx[4];
#pragma nounroll
      for (unsigned sp = 0;; ++sp) {
        asm volatile("" ::: "memory");
        v4u x[4]; unsigned bad = 0;
#pragma unroll
        for (int k = 0; k < 4; ++k) x[k] = ws_ld4(rs, r0, lay + k * 4 * 1792);
        if (L == 0) {
          const unsigned a = L_W2 + trp + (64 * wc + 8 * lg + trl) * 32, ad = L_W2 + trp + 28672 + trl * 896 + (64 * wc + 16 * lg) * 2;
#pragma unroll
          for (int s = 0; s < 2; ++s) { w2f[s] = frag(a + s * 1024, a + s * 1024 + 128); w2f[2 + s] = frag(a + s * 1024 + 14336, a + s * 1024 + 14336 + 128); }
          w2f[4] = frag(ad, ad + 4 * 896); w2f[5] = frag(ad + 8 * 896, ad + 12 * 896);
          v4f cg = mfma(w2f[0], ones, z4); cg = mfma(w2f[1], ones, cg);
          v4f cu = mfma(w2f[2], ones, z4); cu = mfma(w2f[3], ones, cu);
          if (ln < 2) *(LAS v4f*)(r2 + 1008) = ln ? cu : cg;
          SCHED();
        }
#pragma unroll
        for (int k = 0; k < 4; ++k) xx[k] = untag4(x[k], tag, bad);
        if (LIKELY(!anybad(bad))) break;
        asm volatile("" ::: "memory");
        if (STAMPS == 1) ++rt[L];
        if (sp >= SPIN_CAP) break;
      }
      STAMP(7 + 10 * L);
      const float att = tred4((xx[0] + xx[1]) + (xx[2] + xx[3]));
      const float x1 = rbf(xres + rbf(att));
      *(LAS u16*)xs_w = (u16)(f2u(x1) >> 16);
      if (f == 0) ws_st1(rs, r1, lay + WS_X1, f2u(x1) | tag);
    }
    BAR();
    float rd = 0.f, mc = 0.f;
    if (LO || w < 7) {
      const v4u x0 = lds_ld4(xs_r), x1f = lds_ld4(xs_r + 64);
      const v4u xa = lds_ld4(l < 56 ? L_XS + l * 16 : L_ZERO);
      if (L == 0) {
#pragma unroll
        for (int b = 0; b < 7; ++b) cgu += lds_ldf(r3 + 1008 + b * 144);
      }
      v4f ga = mfma(w2f[0], x0, z4); v4f ua = mfma(w2f[2], x0, z4);
      ga = mfma(w2f[1], x1f, ga); ua = mfma(w2f[3], x1f, ua);
      float sx = dot2(xa.x, 0x3f803f80u, 0.f), sxx = dot2(xa.x, xa.x, 0.f);
      sx = dot2(xa.y, 0x3f803f80u, sx); sxx = dot2(xa.y, xa.y, sxx);
      sx = dot2(xa.z, 0x3f803f80u, sx); sxx = dot2(xa.z, xa.z, sxx);
      sx = dot2(xa.w, 0x3f803f80u, sx); sxx = dot2(xa.w, xa.w, sxx);
      if (ln < 2) *(LAS v4f*)r2 = ln ? ua : ga;
      float st = red32(sx, sxx);
      st = row_sum(red16(st, st));
      const float mean = ln_mean(rdlane(st, 0));
      rd = ln_rden(mean, rdlane(st, 32));
      mc = rbf(mean) * cgu;
      asm volatile("" : "+v"(rd), "+v"(mc));
    }
    BAR();
    if (LO || w < 7) {
      float sv[7];
#pragma unroll
      for (int b = 0; b < 7; ++b) sv[b] = lds_ldf(r3 + b * 144);
      const float s = ((sv[0] + sv[1]) + (sv[2] + sv[3])) + ((sv[4] + sv[5]) + (sv[6] - mc));
      const float val = rbf(s * rd);
      auto sw = __builtin_amdgcn_permlane16_swap(f2u(val), f2u(val), false, false);
      const float gg = u2f(sw[0]), uu = u2f(sw[1]);
      const float sg = rbf(gg * __builtin_amdgcn_rcpf(1.0f + __builtin_amdgcn_exp2f(gg * -1.4426950408889634f)));
      const float h = sg * uu;
      const unsigned hp = pk_bf16(h, dpp<QP_X1>(h));
      unsigned hs[8];
#pragma unroll
      for (int k = 0; k < 8; ++k) hs[k] = (unsigned)__builtin_amdgcn_readlane((int)hp, 2 * k);
      asm volatile("" : "+s"(hs[0]), "+s"(hs[1]), "+s"(hs[2]), "+s"(hs[3]), "+s"(hs[4]), "+s"(hs[5]), "+s"(hs[6]), "+s"(hs[7]));
      float d0 = 0.f, d1 = 0.f;
#pragma unroll
      for (int k = 0; k < 4; ++k) { d0 = dot2(w2f[4][k], hs[k], d0); d1 = dot2(w2f[5][k], hs[4 + k], d1); }
      ws_st1(rs, r4, lay, tagv(d0 + d1, tag));
    }
    STAMP(8 + 10 * L);
  };
  auto x_stage = [&](auto Lc) -> float {
    constexpr int L = decltype(Lc)::value; constexpr unsigned lay = L * WS_L;
    float x1;
    if (L == 0 && isF) { x1 = u2f((unsigned)*(LAS u16*)(L_XS + qi * 128 + c4 * 2) << 16); NAP(T_DF); }
    else { x1 = u2f(poll1(rs, x1o, lay, tag)); if (L == 0) NAP(T_D0); else NAP(T_D1); }
    if (L == 0) STAMP(10); else STAMP(21);
    if (LO || w < 7) {
      v4f dx[4];
      read_units<4, 4 * 1792>(rs, dofs, lay, tag, dx, rt[2 + L]);
      if (QH_EARLY && L == 0 && LO && w == 3 && l == 0) ws_st1(rs, 0, WS_L + WS_QHINT + (unsigned)((gq * 4 + qsub) * 7 + qi) * 128u, ep1);
      *(LAS float*)(red_a + w * 256) = tred4((dx[0] + dx[1]) + (dx[2] + dx[3]));
    }
    STAMP(9 + 10 * L);
    BAR();
    if (!QH_EARLY && L == 0 && LO && w == 1 && l == 0) ws_st1(rs, 0, WS_L + WS_QHINT + (unsigned)((gq * 4 + qsub) * 7 + qi) * 128u, ep1);
    float sv[7];
#pragma unroll
    for (int b = 0; b < 7; ++b) sv[b] = lds_ldf(red_a + b * 256);
    const float s = ((sv[0] + sv[1]) + (sv[2] + sv[3])) + ((sv[4] + sv[5]) + sv[6]);
    return rbf(x1 + rbf(s));
  };
  auto read_cq = [&]() {
    v4f cx[2];
    read_2<8>(rs, qo0, lg < 3 ? qo1 : qo0, WS_CDELTA, tag, cx, rt[5]);
    v4f c4v = cx[0]; if (lg < 3) c4v += cx[1];
    cq = tred4(c4v);
  };
  const std::integral_constant<int, 0> L0; const std::integral_constant<int, 1> L1;

  if (LO) {
    NAP(T_QH);
    STAMP(2);
    __builtin_amdgcn_s_setprio(3);
    {
      const unsigned q1 = lg < 3 ? qo1 : qo0;
      v4f qx[4];
#pragma nounroll
      for (unsigned sp = 0;; ++sp) {
        asm volatile("" ::: "memory");
        unsigned bad = 0;
        const v4u x0 = ws_ld4(rs, qo0, 0), x1 = ws_ld4(rs, q1, 0), x2 = ws_ld4(rs, qo0, 1024), x3 = ws_ld4(rs, q1, 1024);
        qx[0] = untag4(x0, tag, bad); qx[1] = untag4(x1, tag, bad); qx[2] = untag4(x2, tag, bad); qx[3] = untag4(x3, tag, bad);
        if (LIKELY(!anybad(bad))) break;
        asm volatile("" ::: "memory");
        if (sp >= SPIN_CAP) break;
      }
    asm volatile("s_waitcnt vmcnt(0)" : "+v"(kf[0]), "+v"(kf[1]), "+v"(kf[2]), "+v"(kf[3]), "+v"(kf[4]), "+v"(kf[5]), "+v"(kf[6]), "+v"(kf[7]),
                 "+v"(kf[8]), "+v"(kf[9]), "+v"(kf[10]), "+v"(kf[11]), "+v"(kf[12]), "+v"(kf[13]), "+v"(kf[14]), "+v"(kf[15]) :: "memory");
      if (lg < 3) { qx[0] += qx[1]; qx[2] += qx[3]; }
      const float U0 = tred4(qx[0]), U1 = tred4(qx[2]);
      *(LAS u16*)q_lw = (u16)pk_bf16(U0, U0);
      *(LAS u16*)(q_lw + 640) = (u16)pk_bf16(U1, U1);
    }
    STAMP(3);
    BAR();
    qk_softmax(true);
    STAMP(4);
    BAR();
    {
      const unsigned a = L_V + (32 * lg + trl) * 512 + trp, h = trl | ((unsigned)(lg & 1) << 2);
#pragma unroll
      for (int tau = 0; tau < 4; ++tau)
#pragma unroll
        for (int t = 0; t < 4; ++t) { const unsigned at = a + (((unsigned)(4 * w + t) ^ h) * 32) + tau * 4096; vf[t * 4 + tau] = frag(at, at + 2048); }
    }
    pv(0);
    STAMP(6);
    __builtin_amdgcn_s_setprio(0);
    if (isF) BAR();
  } else {
    if (isQ) {
      float e[8];
#pragma unroll
      for (int k = 0; k < 4; ++k) { e[2 * k] = u2f(xall[k] << 16); e[2 * k + 1] = u2f(xall[k] & 0xffff0000u); }
      float s = ((e[0] + e[1]) + (e[2] + e[3])) + ((e[4] + e[5]) + (e[6] + e[7]));
      s = rows_sum(row_sum(l < 56 ? s : 0.f));
      const float mean = s * (1.0f / D);
      float s2 = 0.f;
#pragma unroll
      for (int k = 0; k < 8; ++k) { const float d = e[k] - mean; s2 += d * d; }
      s2 = rows_sum(row_sum(l < 56 ? s2 : 0.f));
      const float den = rbf(__builtin_amdgcn_sqrtf(rbf(rbf(s2 * (1.0f / D)) + 1e-5f)));
      float xv = rbf(rbf(u2f(xq_raw << 16) - rbf(mean)) / den);
      asm volatile("s_waitcnt vmcnt(0)" : "+v"(xv) :: "memory");
      const unsigned a = L_WQ + wv * 4096 + (8 * lg + trl) * 64 + trp;
#pragma unroll
      for (int t = 0; t < 2; ++t)
#pragma unroll
        for (int s_ = 0; s_ < 2; ++s_) wqf[2 * t + s_] = frag(a + t * 32 + s_ * 2048, a + t * 32 + s_ * 2048 + 256);
      v4u x0, x1;
      q_mfma(xv, 0, x0, x1);
    }
    if (isQ) {
      v4f c0 = mfma(wqf[0], ones, z4), c1 = mfma(wqf[2], ones, z4);
      c0 = mfma(wqf[1], ones, c0); c1 = mfma(wqf[3], ones, c1);
      if (ln < 2) ws_st4(rs, a0, WS_CDELTA, tag4(ln ? c1 : c0, tag));
    }
    STAMP(2);
    __builtin_amdgcn_s_setprio(0);
    const u16* vb = p.vc + kvrow * 256;
    const unsigned v0 = ((unsigned)(l >> 5) * 512u + (unsigned)(l & 31) * 16u) ^ ((unsigned)(l >> 5) << 5) ^ ((unsigned)(wv & 1) << 7);
#pragma unroll
    for (int u = 0; u < 4; ++u) dma16x4(vb + ((4 * u + rot) & 15) * 2048, v0, v0 ^ 64u, L_V + wv * 16384 + u * 4096);
    STAMP(3);
    BAR();
    const unsigned fvo = (unsigned)(l >> 1) * 3584u + (unsigned)(l & 1) * 16u;
    const u16* fb = ((wv & 1) ? p.wu : p.wg) + (size_t)(224 * (wv >> 1)) * FF + 16 * f;
    const unsigned fl = L_W2 + (wv & 1) * 14336 + (wv >> 1) * 7168;
    read_cq();
    VMWAIT(0);
    STAMP(4);
    BAR();
    if (isF) {
#pragma unroll
      for (int u = 0; u < FW_EARLY; ++u) dma16(fb + (size_t)(32 * u) * FF, fvo, fl + u * 1024);
    }
    if (w == 4) ostat(0);
    if (w == 5) { NAP(T_OH0); if (l == 0) ws_st1(rs, 0, WS_OHINT + orec * 128u, ep1); }
    if (isO) {
      const u16* wob = p.wo + (size_t)(256 * (2 * go + qsub) + 64 * wv) * D + 64 * qi;
      const unsigned ovo = (unsigned)(l >> 3) * 896u + (unsigned)(l & 7) * 16u;
#pragma unroll
      for (int u = 0; u < 8; ++u) dma16(wob + u * 8 * D, ovo, L_W2 + (8 * wv + u) * 1024);
    } else if (isF) {
      BAR();
#pragma unroll
      for (int u = FW_EARLY; u < 7; ++u) dma16(fb + (size_t)(32 * u) * FF, fvo, fl + u * 1024);
#pragma unroll
      for (int u = 0; u < 4; ++u) { const int pc = (4 * u + wv) < 14 ? 4 * u + wv : 13; dma16(p.wd + (size_t)(16 * f) * D + pc * 512, (unsigned)l * 16u, L_W2 + 28672 + pc * 1024); }
    }
    STAMP(6);
  }
  if (LO && !isO) read_cq();
  if (isO) {
    o_stage(L0);
    if (LO) read_cq();
  } else if (isF) {
    f_stage(L0);
  }
  if (isQ) {
    const float xv = x_stage(L0);
    if (!LO) {
      v4u x0, x1;
      q_mfma(xv, WS_L, x0, x1);
    } else if (qsub == 0) {
      if (w == 0) {
        float sx = rows_sum(row_sum(xv)), sxx = rows_sum(row_sum(xv * xv));
        const float h0 = u2f(f2u(sx) & 0xffffff00u), h1 = u2f(f2u(sxx) & 0xffffff00u);
        if (l == 0) ws_st4(rs, 0, WS_L + qstat_off, tag4(v4f{h0, sx - h0, h1, sxx - h1}, tag));
      } else if (w == 2 && g == 0) {
        ws_st1(rs, x1o, WS_X2 - WS_X1, f2u(xv) | tag);
      }
    }
  }
  STAMP(12);
  {
    if ((!NOHINT_Q || !isQ) && LO && w == 1) { wait_hint(rs, (unsigned)(l < 28 ? l : 27) * 128u, WS_L + WS_QHINT + (unsigned)g * 3584u, ep1); NAP(T_Q); }
    BAR();
    if (NOHINT_Q && isQ) NAP(T_XH);
    STAMP(11);
    v4f qx[2];
    read_2<0>(rs, qo0, qo1, WS_L, tag, qx, rt[4]);
    v4f a4v = qx[0]; if (lg < 3) a4v += qx[1];
    const float U = tred4(a4v);
    float sx = (ln < 7) ? qx[1].x + qx[1].y : 0.f;
    sx = row_sum(sx);
    const float mean = ln_mean(rdlane(sx, 48));
    const float u = U - rbf(mean) * cq;
    *(LAS u16*)q_lw = (u16)pk_bf16(u, u);
    STAMP(13);
    BAR();
    if (LO) {
      float sxx = (ln < 7) ? qx[1].z + qx[1].w : 0.f;
      sxx = row_sum(sxx);
      rden = ln_rden(mean, rdlane(sxx, 48)) * (1.4426950408889634f / 16.0f);
      qk_softmax(false);
    } else if (!NOHINT_O && w == 5) { NAP(T_OH1); if (l == 0) ws_st1(rs, 0, WS_L + WS_OHINT + orec * 128u, ep1); }
    STAMP(14);
    BAR();
    if (LO) pv(WS_L); else if (w == 4) ostat(WS_L);
    STAMP(16);
  }
  if (isO) o_stage(L1); else if (isF) f_stage(L1);
  if (g == 0 && j < 7) {
    const float xv = x_stage(L1);
    if (LO && w == 0) {
      p.out[64 * qi + c4] = (u16)(f2u(xv) >> 16);
      if (j == 0 && l == 0) ws_st1(rs, 0, WS_EPOCH, ep1);
    }
    STAMP(20);
  }
}

__global__ void __launch_bounds__(512, 2) fused_decode(Params p) {
  extern __shared__ __attribute__((aligned(16))) unsigned char smem[];
  const int tid = threadIdx.x, l = tid & 63, lg = l >> 4, ln = l & 15;
  int w = __builtin_amdgcn_readfirstlane(tid >> 6);
  const int wv = w & 3, wg = blockIdx.x, g = wg & 7, j = wg >> 3;
  const unsigned c4 = (unsigned)(4 * ln + lg);
  const int qi = j % 7, qsub = j / 7, gq = (g + SHIFT_Q) & 7;
  unsigned epoch, xr_raw, xq_raw; v4u xall, kf[16];
#pragma unroll
  for (int i = 0; i < 16; ++i) asm volatile("" : "=v"(kf[i]));
  if (w >= 4) __builtin_amdgcn_s_setprio(2);
  {
    unsigned z = 0;
    asm volatile("global_load_dwordx4 %0, %1, %2" : "=v"(xall) : "v"((unsigned)(l < 56 ? l : 0) * 16u), "s"(p.x) : "memory");
    asm volatile("global_load_ushort %0, %1, %2" : "=v"(xq_raw) : "v"((64u * qi + c4) * 2u), "s"(p.x) : "memory");
    asm volatile("global_load_ushort %0, %1, %2" : "=v"(xr_raw) : "v"((64u * (w < 7 ? w : 6) + c4) * 2u), "s"(p.x) : "memory");
    asm volatile("global_load_dword %0, %1, %2 sc1" : "=v"(epoch) : "v"(z), "s"(p.ws) : "memory");
  }
  if (w >= 4) {
    if (j < 28) {
      const u16* wqb = p.wq + (size_t)(64 * qi) * 4096 + 512 * gq + 128 * qsub + 32 * wv;
      const unsigned vo = (unsigned)(l >> 2) * 8192u + (unsigned)(l & 3) * 16u;
#pragma unroll
      for (int u = 0; u < 4; ++u) dma16(wqb + u * 16 * 4096, vo, L_WQ + wv * 4096 + u * 1024);
    } else {
      const u64 pc = __builtin_amdgcn_s_getpc() & ~255ull;
#pragma unroll
      for (int u = 0; u < 4; ++u) {
        const unsigned pz = u < 2 ? (unsigned)(((j - 28) * 4 + wv) * 2 + u) * 1024u + (unsigned)l * 16u : 0u;
        dma16<false>((const void*)pc, pz < CODE_PF_BYTES ? pz : 0u, L_WQ + wv * 4096);
      }
    }
  }
  if (w < 4) {
    NAP(T_K);
    const u16* kb0 = p.kc + ((size_t)g * SK + (SK - WIN) + 128 * j) * 256;
    const unsigned koff = (unsigned)(l & 7) * 512u + (unsigned)(l >> 3) * 16u;
    const int rot = wv + j + 5 * g;
#pragma unroll
    for (int T = 0; T < 4; ++T) {
      const u16* kb = kb0 + ((4 * T + rot) & 15) * 2048;
      asm volatile("global_load_dwordx4 %0, %4, %5 nt\n\tglobal_load_dwordx4 %1, %4, %5 offset:128 nt\n\t"
                   "global_load_dwordx4 %2, %4, %5 offset:256 nt\n\tglobal_load_dwordx4 %3, %4, %5 offset:384 nt"
                   : "+v"(kf[4 * T]), "+v"(kf[4 * T + 1]), "+v"(kf[4 * T + 2]), "+v"(kf[4 * T + 3]) : "v"(koff), "s"(kb) : "memory");
    }
  }
  asm volatile("" : "+s"(w));
  if (w < 4) run<true>(p, w, kf, epoch, xr_raw, xq_raw, xall); else run<false>(p, w, kf, epoch, xr_raw, xq_raw, xall);
}
}

extern "C" void kernel_launch(void* const* d_in, const int* in_sizes, int n_in, void* d_out, int out_size, void* d_ws, size_t ws_size,
                              hipStream_t stream) {
  Params p;
  p.x = (const u16*)d_in[0]; p.kc = (const u16*)d_in[1]; p.vc = (const u16*)d_in[2]; p.wq = (const u16*)d_in[3]; p.wo = (const u16*)d_in[4];
  p.wg = (const u16*)d_in[6]; p.wu = (const u16*)d_in[7]; p.wd = (const u16*)d_in[8];
  p.out = (u16*)d_out; p.ws = (unsigned char*)d_ws;
  (void)hipFuncSetAttribute((const void*)fused_decode, hipFuncAttributeMaxDynamicSharedMemorySize, (int)LDS_BYTES);
  hipLaunchKernelGGL(fused_decode, dim3(256), dim3(512), LDS_BYTES, stream, p);
}
```
